```python
import torch

def _softmax(x, axis=-1):
    return torch.softmax(x, dim=axis)
def _gelu(x):
    return torch.nn.functional.gelu(x, approximate="tanh")
def _silu(x):
    return torch.nn.functional.silu(x)
def _sigmoid(x):
    return torch.sigmoid(x)
def _layernorm(x, axis=-1, eps=1e-5):
    m = x.mean(dim=axis, keepdim=True)
    v = x.var(dim=axis, keepdim=True, unbiased=False)
    return (x - m) / torch.sqrt(v + eps)
def _scatter_add(tgt, idx, src, axis=0):
    return tgt.clone().index_add_(axis, idx, src)
def _segment_sum(src, seg, n):
    out = torch.zeros((n,) + src.shape[1:], dtype=src.dtype)
    return out.index_add_(0, seg, src)
def _pad(x, pw):
    flat = tuple(v for pair in reversed(pw) for v in pair)
    return torch.nn.functional.pad(x, flat)

kernel_name = 'standalone__kv_cache_quant_int4_int2_kv_with_per_channel_or_per_token_scales__sampling'

def setup_inputs():

    _w = (lambda a: (lambda t: t.to(torch.bfloat16) if t.is_floating_point() else t)(torch.as_tensor(a)))
    B = 8
    Sq = 32
    Skv = 8192
    Hq = 32
    Hkv = 8
    Dh = 128
    bits = 4
    G = 64
    if G <= 0 or G >= Skv:
        G = max(1, Skv // 2)
    Skv = (Skv // G) * G
    ngk = Skv // G
    qmax = (1 << bits) - 1
    return {
        "q": _w((lambda *s: torch.randn(*s, dtype=torch.bfloat16) * 1.6)(B, Hq, Sq, Dh)),
        "qk": _w(torch.randint(0, qmax + 1, (B, Hkv, Skv, Dh)).to(torch.int32)),
        "qv": _w(torch.randint(0, qmax + 1, (B, Hkv, Skv, Dh)).to(torch.int32)),
        "k_scales": _w(torch.abs((lambda *s: torch.randn(*s, dtype=torch.bfloat16) * 1.6)(B, Hkv, ngk, Dh)) * 0.05 + 0.02),
        "k_zeros": _w((lambda *s: torch.randn(*s, dtype=torch.bfloat16) * 1.6)(B, Hkv, ngk, Dh) + float(qmax) / 2.0),
        "v_scales": _w(torch.abs((lambda *s: torch.randn(*s, dtype=torch.bfloat16) * 0.05)(B, Hkv, Skv, 1)) * 0.05 + 0.02),
        "v_zeros": _w((lambda *s: torch.randn(*s, dtype=torch.bfloat16) * 0.05)(B, Hkv, Skv, 1) + float(qmax) / 2.0),
    }

def reference(q, qk, qv, k_scales, k_zeros, v_scales, v_zeros):
    B, Hq, Sq, Dh = q.shape
    Hkv, Skv = qk.shape[1], qk.shape[2]
    ngk = k_scales.shape[2]
    G = Skv // ngk
    ks = (lambda x, n, axis: torch.repeat_interleave(x, n, dim=axis))(k_scales, G, axis=2)
    kz = (lambda x, n, axis: torch.repeat_interleave(x, n, dim=axis))(k_zeros, G, axis=2)
    k = ((lambda x, dt: x.to(dt))(qk, q.dtype) - kz) * ks
    v = ((lambda x, dt: x.to(dt))(qv, q.dtype) - v_zeros) * v_scales
    rep = Hq // Hkv
    if rep > 1:
        k = (lambda x, n, axis: torch.repeat_interleave(x, n, dim=axis))(k, rep, axis=1)
        v = (lambda x, n, axis: torch.repeat_interleave(x, n, dim=axis))(v, rep, axis=1)
    scale = float(Dh) ** -0.5
    logits = torch.einsum("bhqd,bhkd->bhqk", q, k) * scale
    p = _softmax(logits, axis=-1)
    o = torch.einsum("bhqk,bhkd->bhqd", p, v)
    return o
```

```cpp
#include <hip/hip_runtime.h>
#include <hip/hip_bf16.h>
#include <stdint.h>

namespace kvq {

constexpr int NB = 8, HQ = 32, HKV = 8, SQ = 32, SKV = 8192, DH = 128;
constexpr int TILE = 64;
constexpr int NGK = SKV / TILE;
constexpr int NSPLIT = 4;
constexpr int NBH = NB * HKV;
constexpr int TPS = NGK / NSPLIT;
constexpr int KPS = TPS * TILE;
#define KVQ_DEPTH 3
constexpr int DEPTH = KVQ_DEPTH;

constexpr int KROW = 272;
constexpr int KSLOT = TILE * KROW;
constexpr int VSLOT = 16 * 1024;
constexpr int SLOT = KSLOT + VSLOT;
constexpr int L_KS = 2 * SLOT;
constexpr int L_KZ = L_KS + TPS * DH * 2;
constexpr int L_VS = L_KZ + TPS * DH * 2;
constexpr int L_VZ = L_VS + KPS * 2;
constexpr int LDS_BYTES = L_VZ + KPS * 2;

constexpr size_t WS_PO = 0;
constexpr size_t WS_ML = WS_PO + (size_t)NBH * NSPLIT * 4 * 16 * 64 * 16;

constexpr float SOFTMAX_C = 0.08838834764831845f * 1.4426950408889634f;

typedef int i32x4 __attribute__((ext_vector_type(4)));
typedef unsigned u32x4 __attribute__((ext_vector_type(4)));
typedef unsigned u32x2 __attribute__((ext_vector_type(2)));
typedef float f32x4 __attribute__((ext_vector_type(4)));
typedef float f32x2 __attribute__((ext_vector_type(2)));
typedef float f32x16 __attribute__((ext_vector_type(16)));
typedef __bf16 bf16x8 __attribute__((ext_vector_type(8)));
typedef __bf16 bf16x2 __attribute__((ext_vector_type(2)));

__device__ __forceinline__ float bf_lo(unsigned w) { return __uint_as_float(w << 16); }
__device__ __forceinline__ float bf_hi(unsigned w) { return __uint_as_float(w & 0xffff0000u); }
__device__ __forceinline__ unsigned pk_bf16(float a, float b) {
  f32x2 v = {a, b};
  return __builtin_bit_cast(unsigned, __builtin_convertvector(v, bf16x2));
}
__device__ __forceinline__ float xhalf_max(float x) {
  auto r = __builtin_amdgcn_permlane32_swap(__float_as_uint(x), __float_as_uint(x), false, false);
  return fmaxf(__uint_as_float(r[0]), __uint_as_float(r[1]));
}
__device__ __forceinline__ float xhalf_sum(float x) {
  auto r = __builtin_amdgcn_permlane32_swap(__float_as_uint(x), __float_as_uint(x), false, false);
  return __uint_as_float(r[0]) + __uint_as_float(r[1]);
}

struct Raw { i32x4 k[8]; i32x4 v[8]; };

__device__ __forceinline__ void issue_k(Raw& r, const char* kp, unsigned koff) {
#pragma unroll
  for (int t = 0; t < 8; ++t) r.k[t] = __builtin_nontemporal_load((const i32x4*)(kp + koff + t * 1024));
}
__device__ __forceinline__ void issue_v(Raw& r, const char* vp, unsigned voff) {
#pragma unroll
  for (int j = 0; j < 8; ++j) r.v[j] = __builtin_nontemporal_load((const i32x4*)(vp + voff + j * 512));
}

struct ProdCtx {
  const char* kp; const char* vp;
  unsigned koff, voff;
  char* lds;
  unsigned kw, vw;
  unsigned ksr, vsr;
};

template <bool ISSUE>
__device__ __forceinline__ void produce(const ProdCtx& c, Raw& r, int k) {
  char* slot = c.lds + (k & 1) * SLOT;
  {
    const u32x2 sw = *(const u32x2*)(c.lds + L_KS + c.ksr + k * (DH * 2));
    const u32x2 zw = *(const u32x2*)(c.lds + L_KZ + c.ksr + k * (DH * 2));
    const float s0 = bf_lo(sw[0]), s1 = bf_hi(sw[0]), s2 = bf_lo(sw[1]), s3 = bf_hi(sw[1]);
    const float c0 = -bf_lo(zw[0]) * s0, c1 = -bf_hi(zw[0]) * s1, c2 = -bf_lo(zw[1]) * s2, c3 = -bf_hi(zw[1]) * s3;
#pragma unroll
    for (int t = 0; t < 8; ++t) {
      const i32x4 x = r.k[t];
      u32x2 w;
      w[0] = pk_bf16(fmaf((float)x[0], s0, c0), fmaf((float)x[1], s1, c1));
      w[1] = pk_bf16(fmaf((float)x[2], s2, c2), fmaf((float)x[3], s3, c3));
      *(u32x2*)(slot + c.kw + t * (2 * KROW)) = w;
    }
  }
  __builtin_amdgcn_sched_barrier(0);
  if (ISSUE) issue_k(r, c.kp + (size_t)(k + DEPTH) * (TILE * DH * 4), c.koff);
  __builtin_amdgcn_sched_barrier(0);
  {
    const u32x4 sw = *(const u32x4*)(c.lds + L_VS + c.vsr + k * (TILE * 2));
    const u32x4 zw = *(const u32x4*)(c.lds + L_VZ + c.vsr + k * (TILE * 2));
    u32x4 f0, f1, f2, f3;
#pragma unroll
    for (int jj = 0; jj < 4; ++jj) {
      const float sa = bf_lo(sw[jj]), sb = bf_hi(sw[jj]);
      const float ca = -bf_lo(zw[jj]) * sa, cb = -bf_hi(zw[jj]) * sb;
      const i32x4 xa = r.v[2 * jj], xb = r.v[2 * jj + 1];
      f0[jj] = pk_bf16(fmaf((float)xa[0], sa, ca), fmaf((float)xb[0], sb, cb));
      f1[jj] = pk_bf16(fmaf((float)xa[1], sa, ca), fmaf((float)xb[1], sb, cb));
      f2[jj] = pk_bf16(fmaf((float)xa[2], sa, ca), fmaf((float)xb[2], sb, cb));
      f3[jj] = pk_bf16(fmaf((float)xa[3], sa, ca), fmaf((float)xb[3], sb, cb));
    }
    char* vb = slot + c.vw;
    *(u32x4*)(vb + 0 * 1024) = f0;
    *(u32x4*)(vb + 1 * 1024) = f1;
    *(u32x4*)(vb + 2 * 1024) = f2;
    *(u32x4*)(vb + 3 * 1024) = f3;
  }
  __builtin_amdgcn_sched_barrier(0);
  if (ISSUE) issue_v(r, c.vp + (size_t)(k + DEPTH) * (TILE * DH * 4), c.voff);
  __builtin_amdgcn_sched_barrier(0);
  __syncthreads();
}

template <int K0>
__device__ __forceinline__ void produce_tail(const ProdCtx& c, Raw (&ring)[DEPTH]) {
  if constexpr (K0 < TPS) {
    produce<(K0 + DEPTH < TPS)>(c, ring[K0 % DEPTH], K0);
    produce_tail<K0 + 1>(c, ring);
  }
}

__global__ void __launch_bounds__(512, 2)
attn_split_kernel(const __hip_bfloat16* __restrict__ q, const int* __restrict__ qk, const int* __restrict__ qv,
                  const __hip_bfloat16* __restrict__ ksc, const __hip_bfloat16* __restrict__ kzr,
                  const __hip_bfloat16* __restrict__ vsc, const __hip_bfloat16* __restrict__ vzr,
                  char* __restrict__ ws) {
  extern __shared__ __attribute__((aligned(16))) char lds[];
  const int tid = threadIdx.x, lane = tid & 63;
  const int wave = __builtin_amdgcn_readfirstlane(tid >> 6);
  const int bh = blockIdx.x & (NBH - 1), split = blockIdx.x >> 6;
  const int c = lane & 31, h = lane >> 5;

  if (wave < 4) {
    __builtin_amdgcn_s_setprio(3);
    ProdCtx pc;
    const size_t base = ((size_t)bh * SKV + (size_t)split * KPS) * (DH * 4);
    pc.kp = (const char*)qk + base;
    pc.vp = (const char*)qv + base;
    pc.koff = wave * 8192 + lane * 16;
    pc.voff = wave * 8192 + h * 4096 + c * 16;
    pc.lds = lds;
    pc.kw = (16 * wave + h) * KROW + c * 8;
    pc.vw = KSLOT + wave * 4096 + lane * 16;
    pc.ksr = c * 8;
    pc.vsr = (16 * wave + 8 * h) * 2;
    Raw ring[DEPTH];
#pragma unroll
    for (int d = 0; d < DEPTH; ++d) {
      issue_k(ring[d], pc.kp + (size_t)d * (TILE * DH * 4), pc.koff);
      __builtin_amdgcn_sched_barrier(0);
      issue_v(ring[d], pc.vp + (size_t)d * (TILE * DH * 4), pc.voff);
      __builtin_amdgcn_sched_barrier(0);
    }
    __builtin_amdgcn_sched_barrier(0);
    __syncthreads();
    constexpr int NMAIN = (TPS - DEPTH) / DEPTH;
    for (int it = 0; it < NMAIN; ++it) {
#pragma unroll
      for (int d = 0; d < DEPTH; ++d) produce<true>(pc, ring[d], it * DEPTH + d);
    }
    produce_tail<NMAIN * DEPTH>(pc, ring);
  } else {
    const int n = wave - 4;
    const int ct = tid - 256;
    {
      const size_t kso = ((size_t)bh * NGK + (size_t)split * TPS) * DH * 2;
      const size_t vso = ((size_t)bh * SKV + (size_t)split * KPS) * 2;
      u32x4 a0 = *(const u32x4*)((const char*)ksc + kso + ct * 16), a1 = *(const u32x4*)((const char*)ksc + kso + 4096 + ct * 16);
      u32x4 b0 = *(const u32x4*)((const char*)kzr + kso + ct * 16), b1 = *(const u32x4*)((const char*)kzr + kso + 4096 + ct * 16);
      u32x4 c0 = *(const u32x4*)((const char*)vsc + vso + ct * 16);
      u32x4 d0 = *(const u32x4*)((const char*)vzr + vso + ct * 16);
      *(u32x4*)(lds + L_KS + ct * 16) = a0; *(u32x4*)(lds + L_KS + 4096 + ct * 16) = a1;
      *(u32x4*)(lds + L_KZ + ct * 16) = b0; *(u32x4*)(lds + L_KZ + 4096 + ct * 16) = b1;
      *(u32x4*)(lds + L_VS + ct * 16) = c0;
      *(u32x4*)(lds + L_VZ + ct * 16) = d0;
    }
    bf16x8 qf[8];
    {
      const char* qp = (const char*)q + (((size_t)bh * 4 + n) * SQ + c) * (DH * 2) + h * 16;
#pragma unroll
      for (int s = 0; s < 8; ++s) qf[s] = *(const bf16x8*)(qp + s * 32);
    }
    __syncthreads();

    const int pr = (c & 0x13) | ((c & 4) << 1) | ((c & 8) >> 1);
    const unsigned kr = pr * KROW + h * 16;
    const unsigned vr = KSLOT + lane * 16;
    f32x16 o[4];
#pragma unroll
    for (int i = 0; i < 4; ++i)
#pragma unroll
      for (int g = 0; g < 16; ++g) o[i][g] = 0.f;
    float m = -__builtin_inff(), l = 0.f;

    for (int k = 0; k < TPS; ++k) {
      __syncthreads();
      const char* slot = lds + (k & 1) * SLOT;
      f32x16 s0, s1;
#pragma unroll
      for (int g = 0; g < 16; ++g) { s0[g] = 0.f; s1[g] = 0.f; }
#pragma unroll
      for (int s = 0; s < 8; ++s) {
        const bf16x8 ka = *(const bf16x8*)(slot + kr + s * 32);
        const bf16x8 kb = *(const bf16x8*)(slot + kr + 32 * KROW + s * 32);
        s0 = __builtin_amdgcn_mfma_f32_32x32x16_bf16(ka, qf[s], s0, 0, 0, 0);
        s1 = __builtin_amdgcn_mfma_f32_32x32x16_bf16(kb, qf[s], s1, 0, 0, 0);
      }
      float mx = fmaxf(s0[0], s1[0]);
#pragma unroll
      for (int g = 1; g < 16; ++g) mx = fmaxf(mx, fmaxf(s0[g], s1[g]));
      mx = xhalf_max(mx);
      const float mn = fmaxf(m, mx);
      const float alpha = __builtin_amdgcn_exp2f((m - mn) * SOFTMAX_C);
      m = mn;
      const float mc = -mn * SOFTMAX_C;
      float ps = 0.f;
#pragma unroll
      for (int g = 0; g < 16; ++g) {
        s0[g] = __builtin_amdgcn_exp2f(fmaf(s0[g], SOFTMAX_C, mc));
        s1[g] = __builtin_amdgcn_exp2f(fmaf(s1[g], SOFTMAX_C, mc));
        ps += s0[g] + s1[g];
      }
      l = fmaf(l, alpha, ps);
#pragma unroll
      for (int i = 0; i < 4; ++i)
#pragma unroll
        for (int g = 0; g < 16; ++g) o[i][g] *= alpha;
      u32x4 pf[4];
#pragma unroll
      for (int j = 0; j < 4; ++j) {
        pf[0][j] = pk_bf16(s0[2 * j], s0[2 * j + 1]);
        pf[1][j] = pk_bf16(s0[8 + 2 * j], s0[8 + 2 * j + 1]);
        pf[2][j] = pk_bf16(s1[2 * j], s1[2 * j + 1]);
        pf[3][j] = pk_bf16(s1[8 + 2 * j], s1[8 + 2 * j + 1]);
      }
#pragma unroll
      for (int s = 0; s < 4; ++s)
#pragma unroll
        for (int i = 0; i < 4; ++i) {
          const bf16x8 va = *(const bf16x8*)(slot + vr + (s * 4 + i) * 1024);
          o[i] = __builtin_amdgcn_mfma_f32_32x32x16_bf16(va, __builtin_bit_cast(bf16x8, pf[s]), o[i], 0, 0, 0);
        }
    }
    l = xhalf_sum(l);
    f32x4* po = (f32x4*)(ws + WS_PO) + ((size_t)blockIdx.x * 4 + n) * (16 * 64) + lane;
#pragma unroll
    for (int g = 0; g < 16; ++g) po[g * 64] = f32x4{o[0][g], o[1][g], o[2][g], o[3][g]};
    if (h == 0) ((f32x2*)(ws + WS_ML))[((size_t)blockIdx.x * 4 + n) * 32 + c] = f32x2{m, l};
  }
}

__global__ void __launch_bounds__(256)
combine_kernel(const char* __restrict__ ws, __hip_bfloat16* __restrict__ out) {
  const int lane = threadIdx.x & 63, gq = threadIdx.x >> 6;
  const int bh = blockIdx.x >> 2, n = blockIdx.x & 3;
  const int c = lane & 31, h = lane >> 5;
  f32x2 ml[NSPLIT];
  f32x4 v[NSPLIT][4];
#pragma unroll
  for (int s = 0; s < NSPLIT; ++s) {
    const size_t wn = ((size_t)(s * NBH + bh) * 4 + n);
    ml[s] = ((const f32x2*)(ws + WS_ML))[wn * 32 + c];
    const f32x4* po = (const f32x4*)(ws + WS_PO) + wn * (16 * 64) + gq * 256 + lane;
#pragma unroll
    for (int b = 0; b < 4; ++b) v[s][b] = po[b * 64];
  }
  float M = ml[0][0];
#pragma unroll
  for (int s = 1; s < NSPLIT; ++s) M = fmaxf(M, ml[s][0]);
  float w[NSPLIT], L = 0.f;
#pragma unroll
  for (int s = 0; s < NSPLIT; ++s) { w[s] = __builtin_amdgcn_exp2f((ml[s][0] - M) * SOFTMAX_C); L = fmaf(w[s], ml[s][1], L); }
  const float inv = 1.f / L;
  u32x4 r0, r1;
#pragma unroll
  for (int b = 0; b < 4; ++b) {
    f32x4 a = v[0][b] * w[0];
#pragma unroll
    for (int s = 1; s < NSPLIT; ++s) a += v[s][b] * w[s];
    a *= inv;
    const unsigned p0 = pk_bf16(a[0], a[1]), p1 = pk_bf16(a[2], a[3]);
    if (b < 2) { r0[2 * b] = p0; r0[2 * b + 1] = p1; } else { r1[2 * (b - 2)] = p0; r1[2 * (b - 2) + 1] = p1; }
  }
  char* op = (char*)out + (((size_t)bh * 4 + n) * SQ + c) * (DH * 2) + (32 * gq + 16 * h) * 2;
  *(u32x4*)op = r0;
  *(u32x4*)(op + 16) = r1;
}

}

extern "C" void kernel_launch(void* const* d_in, const int* in_sizes, int n_in, void* d_out, int out_size,
                              void* d_ws, size_t ws_size, hipStream_t stream) {
  using namespace kvq;
  (void)in_sizes; (void)n_in; (void)out_size; (void)ws_size;
  (void)hipFuncSetAttribute((const void*)attn_split_kernel, hipFuncAttributeMaxDynamicSharedMemorySize, LDS_BYTES);
  hipLaunchKernelGGL(attn_split_kernel, dim3(NBH * NSPLIT), dim3(512), LDS_BYTES, stream,
                     (const __hip_bfloat16*)d_in[0], (const int*)d_in[1], (const int*)d_in[2],
                     (const __hip_bfloat16*)d_in[3], (const __hip_bfloat16*)d_in[4],
                     (const __hip_bfloat16*)d_in[5], (const __hip_bfloat16*)d_in[6], (char*)d_ws);
  hipLaunchKernelGGL(combine_kernel, dim3(NBH * 4), dim3(256), 0, stream, (const char*)d_ws, (__hip_bfloat16*)d_out);
}
```

```cpp
#define KVQ_CODEPF 0
#define KVQ_ORDER 2
#include <hip/hip_runtime.h>
#include <hip/hip_bf16.h>
#include <stdint.h>

namespace kvq {

constexpr int NB = 8, HQ = 32, HKV = 8, SQ = 32, SKV = 8192, DH = 128;
constexpr int TILE = 64;
constexpr int NGK = SKV / TILE;
constexpr int NSPLIT = 4;
constexpr int NBH = NB * HKV;
constexpr int TPS = NGK / NSPLIT;
constexpr int KPS = TPS * TILE;
#define KVQ_QNT 1
#define KVQ_SNT 1
#define KVQ_CODEAUX 0
#define KVQ_PACE0 2500
#define KVQ_SPIN 4096
#define KVQ_SPLITLAST 1
constexpr int DEPTH = 2;

constexpr int KROW = 272;
constexpr int KSLOT = TILE * KROW;
constexpr int VSLOT = 16 * 1024;
constexpr int SLOT = KSLOT + VSLOT;
constexpr int L_KS = 2 * SLOT;
constexpr int L_KZ = L_KS + TPS * DH * 2;
constexpr int L_VS = L_KZ + TPS * DH * 2;
constexpr int L_VZ = L_VS + KPS * 2;
constexpr int LDS_BYTES = L_VZ + KPS * 2;

constexpr int WS_CTL = 0;
constexpr int WS_PART = NBH * 4 * 128;
constexpr int PART_ML = 8 * 1024;
constexpr int PART_BYTES = PART_ML + 512;
constexpr int WS_BYTES = WS_PART + NSPLIT * NBH * 4 * PART_BYTES;

constexpr float SOFTMAX_C = 0.08838834764831845f * 1.4426950408889634f;

typedef int i32x4 __attribute__((ext_vector_type(4)));
typedef unsigned u32x4 __attribute__((ext_vector_type(4)));
typedef unsigned u32x2 __attribute__((ext_vector_type(2)));
typedef float f32x4 __attribute__((ext_vector_type(4)));
typedef float f32x2 __attribute__((ext_vector_type(2)));
typedef float f32x16 __attribute__((ext_vector_type(16)));
typedef __bf16 bf16x8 __attribute__((ext_vector_type(8)));
typedef __bf16 bf16x2 __attribute__((ext_vector_type(2)));
typedef unsigned long long u64;
typedef __attribute__((address_space(1))) u64 gu64;
typedef __amdgpu_buffer_rsrc_t rsrc_t;
#define RLX_AGENT __ATOMIC_RELAXED, __HIP_MEMORY_SCOPE_AGENT
constexpr int AUX_SC1 = 16;

#define STAMP(i) do {} while (0)

__device__ __forceinline__ float bf_lo(unsigned w) { return __uint_as_float(w << 16); }
__device__ __forceinline__ float bf_hi(unsigned w) { return __uint_as_float(w & 0xffff0000u); }
__device__ __forceinline__ unsigned pk_bf16(float a, float b) {
  f32x2 v = {a, b};
  return __builtin_bit_cast(unsigned, __builtin_convertvector(v, bf16x2));
}
__device__ __forceinline__ float xhalf_max(float x) {
  auto r = __builtin_amdgcn_permlane32_swap(__float_as_uint(x), __float_as_uint(x), false, false);
  return fmaxf(__uint_as_float(r[0]), __uint_as_float(r[1]));
}
__device__ __forceinline__ float xhalf_sum(float x) {
  auto r = __builtin_amdgcn_permlane32_swap(__float_as_uint(x), __float_as_uint(x), false, false);
  return __uint_as_float(r[0]) + __uint_as_float(r[1]);
}
template <int NT> __device__ __forceinline__ u32x4 ld16(const void* p) {
  if constexpr (NT) return __builtin_nontemporal_load((const u32x4*)p); else return *(const u32x4*)p;
}
__device__ __forceinline__ u64 uniform64(u64 x) {
  const unsigned lo = __builtin_amdgcn_readfirstlane((unsigned)x), hi = __builtin_amdgcn_readfirstlane((unsigned)(x >> 32));
  return ((u64)hi << 32) | lo;
}

struct Raw { i32x4 k[8]; i32x4 v[8]; };

__device__ __forceinline__ void issue_k(Raw& r, const char* kp, unsigned koff) {
#pragma unroll
  for (int t = 0; t < 8; ++t) r.k[t] = __builtin_nontemporal_load((const i32x4*)(kp + koff + t * 1024));
}
__device__ __forceinline__ void issue_v(Raw& r, const char* vp, unsigned voff) {
#pragma unroll
  for (int j = 0; j < 8; ++j) r.v[j] = __builtin_nontemporal_load((const i32x4*)(vp + voff + j * 512));
}

struct ProdCtx {
  const char* kp; const char* vp;
  unsigned koff, voff;
  char* lds;
  unsigned kw, vw;
  unsigned ksr, vsr;
};

template <int SL, bool ISSUE, bool MID>
__device__ __forceinline__ void produce(const ProdCtx& c, Raw& r, int k) {
  char* slot = c.lds + SL * SLOT;
  {
    const u32x2 sw = *(const u32x2*)(c.lds + L_KS + c.ksr + k * (DH * 2));
    const u32x2 zw = *(const u32x2*)(c.lds + L_KZ + c.ksr + k * (DH * 2));
    const float s0 = bf_lo(sw[0]), s1 = bf_hi(sw[0]), s2 = bf_lo(sw[1]), s3 = bf_hi(sw[1]);
    const float c0 = -bf_lo(zw[0]) * s0, c1 = -bf_hi(zw[0]) * s1, c2 = -bf_lo(zw[1]) * s2, c3 = -bf_hi(zw[1]) * s3;
#pragma unroll
    for (int t = 0; t < 8; ++t) {
      const i32x4 x = r.k[t];
      u32x2 w;
      w[0] = pk_bf16(fmaf((float)x[0], s0, c0), fmaf((float)x[1], s1, c1));
      w[1] = pk_bf16(fmaf((float)x[2], s2, c2), fmaf((float)x[3], s3, c3));
      *(u32x2*)(slot + c.kw + t * (2 * KROW)) = w;
    }
  }
  __builtin_amdgcn_sched_barrier(0);
  if (ISSUE && KVQ_ORDER != 1) issue_k(r, c.kp + (size_t)(k + DEPTH) * (TILE * DH * 4), c.koff);
  if (MID) __syncthreads();
  __builtin_amdgcn_sched_barrier(0);
  {
    const u32x4 sw = *(const u32x4*)(c.lds + L_VS + c.vsr + k * (TILE * 2));
    const u32x4 zw = *(const u32x4*)(c.lds + L_VZ + c.vsr + k * (TILE * 2));
    u32x4 f0, f1, f2, f3;
#pragma unroll
    for (int jj = 0; jj < 4; ++jj) {
      const float sa = bf_lo(sw[jj]), sb = bf_hi(sw[jj]);
      const float ca = -bf_lo(zw[jj]) * sa, cb = -bf_hi(zw[jj]) * sb;
      const i32x4 xa = r.v[2 * jj], xb = r.v[2 * jj + 1];
      f0[jj] = pk_bf16(fmaf((float)xa[0], sa, ca), fmaf((float)xb[0], sb, cb));
      f1[jj] = pk_bf16(fmaf((float)xa[1], sa, ca), fmaf((float)xb[1], sb, cb));
      f2[jj] = pk_bf16(fmaf((float)xa[2], sa, ca), fmaf((float)xb[2], sb, cb));
      f3[jj] = pk_bf16(fmaf((float)xa[3], sa, ca), fmaf((float)xb[3], sb, cb));
    }
    char* vb = slot + c.vw;
    *(u32x4*)(vb + 0 * 1024) = f0;
    *(u32x4*)(vb + 1 * 1024) = f1;
    *(u32x4*)(vb + 2 * 1024) = f2;
    *(u32x4*)(vb + 3 * 1024) = f3;
  }
  __builtin_amdgcn_sched_barrier(0);
  if (KVQ_ORDER != 0) __syncthreads();
  __builtin_amdgcn_sched_barrier(0);
  if (ISSUE && KVQ_ORDER == 1) issue_k(r, c.kp + (size_t)(k + DEPTH) * (TILE * DH * 4), c.koff);
  __builtin_amdgcn_sched_barrier(0);
  if (ISSUE) issue_v(r, c.vp + (size_t)(k + DEPTH) * (TILE * DH * 4), c.voff);
  __builtin_amdgcn_sched_barrier(0);
  if (KVQ_ORDER == 0) __syncthreads();
}

struct ConsState { f32x16 o[4]; float m, l; };

template <bool MID>
__device__ __forceinline__ void consume(ConsState& st, const char* slot, const bf16x8 (&qf)[8], unsigned kr, unsigned vr) {
  f32x16 s0, s1;
#pragma unroll
  for (int g = 0; g < 16; ++g) { s0[g] = 0.f; s1[g] = 0.f; }
#pragma unroll
  for (int s = 0; s < 8; ++s) {
    const bf16x8 ka = *(const bf16x8*)(slot + kr + s * 32);
    const bf16x8 kb = *(const bf16x8*)(slot + kr + 32 * KROW + s * 32);
    s0 = __builtin_amdgcn_mfma_f32_32x32x16_bf16(ka, qf[s], s0, 0, 0, 0);
    s1 = __builtin_amdgcn_mfma_f32_32x32x16_bf16(kb, qf[s], s1, 0, 0, 0);
  }
  float mx = fmaxf(s0[0], s1[0]);
#pragma unroll
  for (int g = 1; g < 16; ++g) mx = fmaxf(mx, fmaxf(s0[g], s1[g]));
  mx = xhalf_max(mx);
  const float mn = fmaxf(st.m, mx);
  const float alpha = __builtin_amdgcn_exp2f((st.m - mn) * SOFTMAX_C);
  st.m = mn;
  const float mc = -mn * SOFTMAX_C;
  float ps = 0.f;
#pragma unroll
  for (int g = 0; g < 16; ++g) {
    s0[g] = __builtin_amdgcn_exp2f(fmaf(s0[g], SOFTMAX_C, mc));
    s1[g] = __builtin_amdgcn_exp2f(fmaf(s1[g], SOFTMAX_C, mc));
    ps += s0[g] + s1[g];
  }
  st.l = fmaf(st.l, alpha, ps);
#pragma unroll
  for (int i = 0; i < 4; ++i)
#pragma unroll
    for (int g = 0; g < 16; ++g) st.o[i][g] *= alpha;
  u32x4 pf[4];
#pragma unroll
  for (int j = 0; j < 4; ++j) {
    pf[0][j] = pk_bf16(s0[2 * j], s0[2 * j + 1]);
    pf[1][j] = pk_bf16(s0[8 + 2 * j], s0[8 + 2 * j + 1]);
    pf[2][j] = pk_bf16(s1[2 * j], s1[2 * j + 1]);
    pf[3][j] = pk_bf16(s1[8 + 2 * j], s1[8 + 2 * j + 1]);
  }
  if (MID) { __builtin_amdgcn_sched_barrier(0); __syncthreads(); }
#pragma unroll
  for (int s = 0; s < 4; ++s)
#pragma unroll
    for (int i = 0; i < 4; ++i) {
      const bf16x8 va = *(const bf16x8*)(slot + vr + (s * 4 + i) * 1024);
      st.o[i] = __builtin_amdgcn_mfma_f32_32x32x16_bf16(va, __builtin_bit_cast(bf16x8, pf[s]), st.o[i], 0, 0, 0);
    }
}

__device__ __forceinline__ float merge4(float a, float wa, float b, float wb, float c, float wc, float d, float wd) {
#pragma clang fp contract(off)
  const float p = a * wa, q = b * wb, r = c * wc, s = d * wd;
  return (p + q) + (r + s);
}
__device__ __forceinline__ float sum4(float p, float q, float r, float s) { return (p + q) + (r + s); }

__global__ void __launch_bounds__(512, 2)
attn_kernel(const __hip_bfloat16* __restrict__ q, const int* __restrict__ qk, const int* __restrict__ qv,
            const __hip_bfloat16* __restrict__ ksc, const __hip_bfloat16* __restrict__ kzr,
            const __hip_bfloat16* __restrict__ vsc, const __hip_bfloat16* __restrict__ vzr,
            __hip_bfloat16* __restrict__ out, char* ws) {
  extern __shared__ __attribute__((aligned(16))) char lds[];
  const int tid = threadIdx.x, lane = tid & 63;
  const int wave = __builtin_amdgcn_readfirstlane(tid >> 6);
  const int bh = blockIdx.x & (NBH - 1), split = blockIdx.x >> 6;
  const int c = lane & 31, h = lane >> 5;

  if (wave < 4) {
    __builtin_amdgcn_s_setprio(3);
    ProdCtx pc;
    const size_t base = ((size_t)bh * SKV + (size_t)split * KPS) * (DH * 4);
    pc.kp = (const char*)qk + base;
    pc.vp = (const char*)qv + base;
    pc.koff = wave * 8192 + lane * 16;
    pc.voff = wave * 8192 + h * 4096 + c * 16;
    pc.lds = lds;
    pc.kw = (16 * wave + h) * KROW + c * 8;
    pc.vw = KSLOT + wave * 4096 + lane * 16;
    pc.ksr = c * 8;
    pc.vsr = (16 * wave + 8 * h) * 2;
    Raw r0, r1;
    __syncthreads();
    issue_k(r0, pc.kp, pc.koff); __builtin_amdgcn_sched_barrier(0);
    issue_v(r0, pc.vp, pc.voff); __builtin_amdgcn_sched_barrier(0);
    issue_k(r1, pc.kp + TILE * DH * 4, pc.koff); __builtin_amdgcn_sched_barrier(0);
    issue_v(r1, pc.vp + TILE * DH * 4, pc.voff); __builtin_amdgcn_sched_barrier(0);
    __syncthreads();
    for (int k = 0; k < TPS - 2; k += 2) {
      produce<0, true, false>(pc, r0, k);
      produce<1, true, false>(pc, r1, k + 1);
    }
    produce<0, false, false>(pc, r0, TPS - 2);
    produce<1, false, KVQ_SPLITLAST>(pc, r1, TPS - 1);
  } else {
    const int n = wave - 4;
    const int ct = tid - 256;
    STAMP(0);
    gu64* const ew = (gu64*)(ws + WS_CTL + (bh * 4 + n) * 128);
    gu64* const cw = ew + 8;
    const size_t kso = ((size_t)bh * NGK + (size_t)split * TPS) * DH * 2;
    const size_t vso = ((size_t)bh * SKV + (size_t)split * KPS) * 2;
    u32x4 a0 = ld16<KVQ_SNT>((const char*)ksc + kso + ct * 16), b0 = ld16<KVQ_SNT>((const char*)kzr + kso + ct * 16);
    u32x4 c0 = ld16<KVQ_SNT>((const char*)vsc + vso + ct * 16), d0 = ld16<KVQ_SNT>((const char*)vzr + vso + ct * 16);
    u32x4 a1 = ld16<KVQ_SNT>((const char*)ksc + kso + 4096 + ct * 16), b1 = ld16<KVQ_SNT>((const char*)kzr + kso + 4096 + ct * 16);
    __builtin_amdgcn_sched_barrier(0);
    bf16x8 qf[8];
    {
      const char* qp = (const char*)q + (((size_t)bh * 4 + n) * SQ + c) * (DH * 2) + h * 16;
#pragma unroll
      for (int s = 0; s < 8; ++s) qf[s] = __builtin_bit_cast(bf16x8, ld16<KVQ_QNT>(qp + s * 32));
    }
    __builtin_amdgcn_sched_barrier(0);
    u64 e0v = __hip_atomic_load(ew, RLX_AGENT);
    u64 c0v = __hip_atomic_load(cw, RLX_AGENT);
    __builtin_amdgcn_sched_barrier(0);
    __syncthreads();
    __builtin_amdgcn_sched_barrier(0);
    *(u32x4*)(lds + L_KS + ct * 16) = a0; *(u32x4*)(lds + L_KZ + ct * 16) = b0;
    *(u32x4*)(lds + L_VS + ct * 16) = c0; *(u32x4*)(lds + L_VZ + ct * 16) = d0;
    *(u32x4*)(lds + L_KS + 4096 + ct * 16) = a1; *(u32x4*)(lds + L_KZ + 4096 + ct * 16) = b1;
    __syncthreads();
    STAMP(1);

    const int pr = (c & 0x13) | ((c & 4) << 1) | ((c & 8) >> 1);
    const unsigned kr = pr * KROW + h * 16;
    const unsigned vr = KSLOT + lane * 16;
    ConsState st;
#pragma unroll
    for (int i = 0; i < 4; ++i)
#pragma unroll
      for (int g = 0; g < 16; ++g) st.o[i][g] = 0.f;
    st.m = -__builtin_inff(); st.l = 0.f;

    __syncthreads();
    STAMP(2);
    consume<false>(st, lds, qf, kr, vr);
    __builtin_amdgcn_sched_barrier(0);
    asm volatile("" : "+v"(e0v), "+v"(c0v));
    const u64 epoch = uniform64(e0v);
    const u64 tag = ((epoch + 1) * 0x9E3779B97F4A7C15ull) & ~15ull;
    {
      u64 cur = uniform64(c0v);
      const bool mine = (cur & ~15ull) == tag && (cur & 15ull) != 15ull;
      if (!mine && lane == 0) __hip_atomic_compare_exchange_strong(cw, &cur, tag, __ATOMIC_RELAXED, RLX_AGENT);
    }
    for (int k = 1; k < TPS - 1; ++k) {
      __syncthreads();
      consume<false>(st, lds + (k & 1) * SLOT, qf, kr, vr);
    }
    __syncthreads();
    STAMP(3);
    consume<KVQ_SPLITLAST>(st, lds + ((TPS - 1) & 1) * SLOT, qf, kr, vr);

    st.l = xhalf_sum(st.l);
    u32x4 own[8];
    {
      const float il = 1.f / st.l;
#pragma unroll
      for (int e = 0; e < 8; ++e)
#pragma unroll
        for (int b = 0; b < 2; ++b) {
          const int g = e * 2 + b;
          own[e][2 * b] = pk_bf16(st.o[0][g] * il, st.o[1][g] * il);
          own[e][2 * b + 1] = pk_bf16(st.o[2][g] * il, st.o[3][g] * il);
        }
    }
    STAMP(4);
    const rsrc_t rs = __builtin_amdgcn_make_buffer_rsrc(ws, 0, WS_BYTES, 0x00020000);
    const int rec0 = WS_PART + (bh * 4 + n) * (NSPLIT * PART_BYTES);
    bool merge = false;
    const bool owner = split == n;
    if (owner) {
      for (int spin = 0; spin < KVQ_SPIN; ++spin) {
        if ((unsigned)__builtin_amdgcn_readfirstlane((unsigned)__hip_atomic_load(cw, RLX_AGENT)) == ((unsigned)tag | 3u)) { merge = true; break; }
        __builtin_amdgcn_s_sleep(1);
      }
    }
    if (!merge) {
      const int rec = rec0 + split * PART_BYTES;
#pragma unroll
      for (int e = 0; e < 8; ++e) __builtin_amdgcn_raw_buffer_store_b128(own[e], rs, lane * 16 + e * 1024, rec, AUX_SC1);
      const f32x2 ml = {st.m, st.l};
      __builtin_amdgcn_raw_buffer_store_b64(__builtin_bit_cast(u32x2, ml), rs, lane * 8 + PART_ML, rec, AUX_SC1);
      asm volatile("s_waitcnt vmcnt(0)" ::: "memory");
      u64 old = 0;
      if (lane == 0) old = __hip_atomic_fetch_add(cw, owner ? 8ull : 1ull, RLX_AGENT);
      const unsigned o4 = __builtin_amdgcn_readfirstlane((unsigned)old) & 15u;
      merge = o4 == (owner ? 3u : 10u);
    }
    STAMP(5);
    if (merge) {
      __builtin_amdgcn_fence(__ATOMIC_ACQUIRE, "agent");
      const int rp = rec0 + (split ^ 1) * PART_BYTES;
      const int ra = rec0 + ((split & 2) ^ 2) * PART_BYTES;
      const int rb = ra + PART_BYTES;
      const f32x2 mlp = __builtin_bit_cast(f32x2, __builtin_amdgcn_raw_buffer_load_b64(rs, lane * 8 + PART_ML, rp, AUX_SC1));
      const f32x2 mla = __builtin_bit_cast(f32x2, __builtin_amdgcn_raw_buffer_load_b64(rs, lane * 8 + PART_ML, ra, AUX_SC1));
      const f32x2 mlb = __builtin_bit_cast(f32x2, __builtin_amdgcn_raw_buffer_load_b64(rs, lane * 8 + PART_ML, rb, AUX_SC1));
      u32x4 xp[8], xa[8], xb[8];
#pragma unroll
      for (int e = 0; e < 8; ++e) {
        xp[e] = __builtin_amdgcn_raw_buffer_load_b128(rs, lane * 16 + e * 1024, rp, AUX_SC1);
        xa[e] = __builtin_amdgcn_raw_buffer_load_b128(rs, lane * 16 + e * 1024, ra, AUX_SC1);
        xb[e] = __builtin_amdgcn_raw_buffer_load_b128(rs, lane * 16 + e * 1024, rb, AUX_SC1);
      }
      __builtin_amdgcn_sched_barrier(0);
      const float M = fmaxf(fmaxf(st.m, mlp[0]), fmaxf(mla[0], mlb[0]));
      float wo = __builtin_amdgcn_exp2f((st.m - M) * SOFTMAX_C) * st.l, wp = __builtin_amdgcn_exp2f((mlp[0] - M) * SOFTMAX_C) * mlp[1];
      float wa = __builtin_amdgcn_exp2f((mla[0] - M) * SOFTMAX_C) * mla[1], wb = __builtin_amdgcn_exp2f((mlb[0] - M) * SOFTMAX_C) * mlb[1];
      asm volatile("" : "+v"(wo), "+v"(wp), "+v"(wa), "+v"(wb));
      const float inv = 1.f / sum4(wo, wp, wa, wb);
      wo *= inv; wp *= inv; wa *= inv; wb *= inv;
      char* op = (char*)out + (((size_t)bh * 4 + n) * SQ + c) * (DH * 2) + h * 32;
#pragma unroll
      for (int e = 0; e < 8; ++e) {
        u32x4 r;
#pragma unroll
        for (int j = 0; j < 4; ++j)
          r[j] = pk_bf16(merge4(bf_lo(own[e][j]), wo, bf_lo(xp[e][j]), wp, bf_lo(xa[e][j]), wa, bf_lo(xb[e][j]), wb),
                         merge4(bf_hi(own[e][j]), wo, bf_hi(xp[e][j]), wp, bf_hi(xa[e][j]), wa, bf_hi(xb[e][j]), wb));
        *(u32x4*)(op + (e >> 1) * 64 + (e & 1) * 16) = r;
      }
      if (lane == 0) { __hip_atomic_store(cw, tag | 15ull, RLX_AGENT); __hip_atomic_store(ew, epoch + 1, RLX_AGENT); }
      STAMP(6);
      STAMP(7);
    }
  }
}

}

extern "C" void kernel_launch(void* const* d_in, const int* in_sizes, int n_in, void* d_out, int out_size,
                              void* d_ws, size_t ws_size, hipStream_t stream) {
  using namespace kvq;
  (void)in_sizes; (void)n_in; (void)out_size; (void)ws_size;
  (void)hipFuncSetAttribute((const void*)attn_kernel, hipFuncAttributeMaxDynamicSharedMemorySize, LDS_BYTES);
  hipLaunchKernelGGL(attn_kernel, dim3(NBH * NSPLIT), dim3(512), LDS_BYTES, stream,
                     (const __hip_bfloat16*)d_in[0], (const int*)d_in[1], (const int*)d_in[2],
                     (const __hip_bfloat16*)d_in[3], (const __hip_bfloat16*)d_in[4],
                     (const __hip_bfloat16*)d_in[5], (const __hip_bfloat16*)d_in[6],
                     (__hip_bfloat16*)d_out, (char*)d_ws);
}
```

```cpp
#include <hip/hip_runtime.h>
#include <hip/hip_bf16.h>
#include <stdint.h>

namespace kvq {

constexpr int NB = 8, HQ = 32, HKV = 8, SQ = 32, SKV = 8192, DH = 128;
constexpr int TILE = 64;
constexpr int NGK = SKV / TILE;
constexpr int NSPLIT = 4;
constexpr int NBH = NB * HKV;
constexpr int TPS = NGK / NSPLIT;
constexpr int KPS = TPS * TILE;
#define KVQ_QNT 1
#define KVQ_SNT 1
#define KVQ_SPIN 4096
#define KVQ_ACQ 0
constexpr int DEPTH = 2;

constexpr int KROW = 272;
constexpr int L_K = 0;
constexpr int L_V = TILE * KROW;
constexpr int L_KS = L_V + 16 * 1024;
constexpr int L_KZ = L_KS + TPS * DH * 2;
constexpr int L_VS = L_KZ + TPS * DH * 2;
constexpr int L_VZ = L_VS + KPS * 2;
constexpr int L_OWN = L_VZ + KPS * 2;
constexpr int LDS_BYTES = L_OWN + 8 * 1024 + 512 + 16;

constexpr int WS_CTL = 0;
constexpr int WS_PART = NBH * 4 * 128;
constexpr int PART_ML = 8 * 1024;
constexpr int PART_BYTES = PART_ML + 512;
constexpr int WS_BYTES = WS_PART + NSPLIT * NBH * 4 * PART_BYTES;

constexpr float SOFTMAX_C = 0.08838834764831845f * 1.4426950408889634f;

typedef int i32x4 __attribute__((ext_vector_type(4)));
typedef unsigned u32x4 __attribute__((ext_vector_type(4)));
typedef unsigned u32x2 __attribute__((ext_vector_type(2)));
typedef float f32x2 __attribute__((ext_vector_type(2)));
typedef float f32x16 __attribute__((ext_vector_type(16)));
typedef __bf16 bf16x8 __attribute__((ext_vector_type(8)));
typedef __bf16 bf16x2 __attribute__((ext_vector_type(2)));
typedef unsigned long long u64;
typedef __attribute__((address_space(1))) u64 gu64;
typedef __amdgpu_buffer_rsrc_t rsrc_t;
#define RLX_AGENT __ATOMIC_RELAXED, __HIP_MEMORY_SCOPE_AGENT
#define SB() __builtin_amdgcn_sched_barrier(0)
constexpr int AUX_SC1 = 16;

#define STAMP(i) do {} while (0)

__device__ __forceinline__ float bf_lo(unsigned w) { return __uint_as_float(w << 16); }
__device__ __forceinline__ float bf_hi(unsigned w) { return __uint_as_float(w & 0xffff0000u); }
__device__ __forceinline__ unsigned pk_bf16(float a, float b) {
  f32x2 v = {a, b};
  return __builtin_bit_cast(unsigned, __builtin_convertvector(v, bf16x2));
}
__device__ __forceinline__ float xhalf_max(float x) {
  auto r = __builtin_amdgcn_permlane32_swap(__float_as_uint(x), __float_as_uint(x), false, false);
  return fmaxf(__uint_as_float(r[0]), __uint_as_float(r[1]));
}
__device__ __forceinline__ float xhalf_sum(float x) {
  auto r = __builtin_amdgcn_permlane32_swap(__float_as_uint(x), __float_as_uint(x), false, false);
  return __uint_as_float(r[0]) + __uint_as_float(r[1]);
}
template <int NT> __device__ __forceinline__ u32x4 ld16(const void* p) {
  if constexpr (NT) return __builtin_nontemporal_load((const u32x4*)p); else return *(const u32x4*)p;
}
__device__ __forceinline__ u64 uniform64(u64 x) {
  const unsigned lo = __builtin_amdgcn_readfirstlane((unsigned)x), hi = __builtin_amdgcn_readfirstlane((unsigned)(x >> 32));
  return ((u64)hi << 32) | lo;
}

struct Raw { i32x4 k[8]; i32x4 v[8]; };

__device__ __forceinline__ void issue_k(Raw& r, const char* kp, unsigned koff) {
#pragma unroll
  for (int t = 0; t < 8; ++t) r.k[t] = __builtin_nontemporal_load((const i32x4*)(kp + koff + t * 1024));
}
__device__ __forceinline__ void issue_v(Raw& r, const char* vp, unsigned voff) {
#pragma unroll
  for (int j = 0; j < 8; ++j) r.v[j] = __builtin_nontemporal_load((const i32x4*)(vp + voff + j * 512));
}

struct ProdCtx {
  const char* kp; const char* vp;
  unsigned koff, voff;
  char* lds;
  unsigned kw, vw;
  unsigned ksr, vsr;
};

template <bool ISSUE>
__device__ __forceinline__ void produce(const ProdCtx& c, Raw& r, int k) {
  {
    const u32x2 sw = *(const u32x2*)(c.lds + L_KS + c.ksr + k * (DH * 2));
    const u32x2 zw = *(const u32x2*)(c.lds + L_KZ + c.ksr + k * (DH * 2));
    const float s0 = bf_lo(sw[0]), s1 = bf_hi(sw[0]), s2 = bf_lo(sw[1]), s3 = bf_hi(sw[1]);
    const float c0 = -bf_lo(zw[0]) * s0, c1 = -bf_hi(zw[0]) * s1, c2 = -bf_lo(zw[1]) * s2, c3 = -bf_hi(zw[1]) * s3;
#pragma unroll
    for (int t = 0; t < 8; ++t) {
      const i32x4 x = r.k[t];
      u32x2 w;
      w[0] = pk_bf16(fmaf((float)x[0], s0, c0), fmaf((float)x[1], s1, c1));
      w[1] = pk_bf16(fmaf((float)x[2], s2, c2), fmaf((float)x[3], s3, c3));
      *(u32x2*)(c.lds + c.kw + t * (2 * KROW)) = w;
    }
  }
  SB(); __syncthreads(); SB();
  if (ISSUE) issue_k(r, c.kp + (size_t)(k + DEPTH) * (TILE * DH * 4), c.koff);
  SB();
  {
    const u32x4 sw = *(const u32x4*)(c.lds + L_VS + c.vsr + k * (TILE * 2));
    const u32x4 zw = *(const u32x4*)(c.lds + L_VZ + c.vsr + k * (TILE * 2));
    u32x4 f0, f1, f2, f3;
#pragma unroll
    for (int jj = 0; jj < 4; ++jj) {
      const float sa = bf_lo(sw[jj]), sb = bf_hi(sw[jj]);
      const float ca = -bf_lo(zw[jj]) * sa, cb = -bf_hi(zw[jj]) * sb;
      const i32x4 xa = r.v[2 * jj], xb = r.v[2 * jj + 1];
      f0[jj] = pk_bf16(fmaf((float)xa[0], sa, ca), fmaf((float)xb[0], sb, cb));
      f1[jj] = pk_bf16(fmaf((float)xa[1], sa, ca), fmaf((float)xb[1], sb, cb));
      f2[jj] = pk_bf16(fmaf((float)xa[2], sa, ca), fmaf((float)xb[2], sb, cb));
      f3[jj] = pk_bf16(fmaf((float)xa[3], sa, ca), fmaf((float)xb[3], sb, cb));
    }
    char* vb = c.lds + c.vw;
    *(u32x4*)(vb + 0 * 1024) = f0;
    *(u32x4*)(vb + 1 * 1024) = f1;
    *(u32x4*)(vb + 2 * 1024) = f2;
    *(u32x4*)(vb + 3 * 1024) = f3;
  }
  SB(); __syncthreads(); SB();
  if (ISSUE) issue_v(r, c.vp + (size_t)(k + DEPTH) * (TILE * DH * 4), c.voff);
  SB();
}

struct ConsState { f32x16 o[4]; float m, l; };

constexpr float RESC_THR = 8.0f / SOFTMAX_C;
__device__ __forceinline__ float block_max(const f32x16& s) {
  float a = fmaxf(fmaxf(s[0], s[1]), s[2]), b = fmaxf(fmaxf(s[3], s[4]), s[5]);
  a = fmaxf(fmaxf(a, s[6]), s[7]); b = fmaxf(fmaxf(b, s[8]), s[9]);
  a = fmaxf(fmaxf(a, s[10]), s[11]); b = fmaxf(fmaxf(b, s[12]), s[13]);
  a = fmaxf(fmaxf(a, s[14]), s[15]);
  return xhalf_max(fmaxf(a, b));
}
template <bool PEND>
__device__ __forceinline__ void guard(ConsState& st, float mx, f32x16& pend) {
  if (__builtin_expect(__builtin_amdgcn_ballot_w64(!(mx - st.m <= RESC_THR)) != 0ull, 0)) {
    const float mn = fmaxf(st.m, mx);
    const float alpha = __builtin_amdgcn_exp2f((st.m - mn) * SOFTMAX_C);
    st.m = mn;
    st.l *= alpha;
#pragma unroll
    for (int i = 0; i < 4; ++i)
#pragma unroll
      for (int g = 0; g < 16; ++g) st.o[i][g] *= alpha;
    if (PEND) {
#pragma unroll
      for (int g = 0; g < 16; ++g) pend[g] *= alpha;
    }
  }
}
__device__ __forceinline__ bf16x8 ldsf(const char* p) { return *(const bf16x8*)p; }
__device__ __forceinline__ f32x16 mfma(bf16x8 a, bf16x8 b, f32x16 c) { return __builtin_amdgcn_mfma_f32_32x32x16_bf16(a, b, c, 0, 0, 0); }
__device__ __forceinline__ bf16x8 pack8(const f32x16& p, int o) {
  u32x4 w = {pk_bf16(p[o], p[o + 1]), pk_bf16(p[o + 2], p[o + 3]), pk_bf16(p[o + 4], p[o + 5]), pk_bf16(p[o + 6], p[o + 7])};
  return __builtin_bit_cast(bf16x8, w);
}
#define EXP2(S, g) do { S[g] = __builtin_amdgcn_exp2f(fmaf(S[g], SOFTMAX_C, mc)); S[(g) + 1] = __builtin_amdgcn_exp2f(fmaf(S[(g) + 1], SOFTMAX_C, mc)); } while (0)
#define SUM2(S, g) do { ls += S[g]; ls += S[(g) + 1]; } while (0)
#define PIN2(S, g) do { float e0_ = S[g], e1_ = S[(g) + 1]; asm volatile("" : "+v"(e0_), "+v"(e1_), "+v"(ls)); S[g] = e0_; S[(g) + 1] = e1_; } while (0)

__device__ __forceinline__ void consume(ConsState& st, const char* lds, const bf16x8 (&qf)[8], unsigned kr, unsigned vr) {
  f32x16 s0, s1;
#pragma unroll
  for (int g = 0; g < 16; ++g) { s0[g] = 0.f; s1[g] = 0.f; }
  bf16x8 fa[8], fb[8];
  SB(); __syncthreads(); SB();
#pragma unroll
  for (int s = 0; s < 8; ++s) fa[s] = ldsf(lds + kr + s * 32);
#pragma unroll
  for (int s = 0; s < 8; ++s) fb[s] = ldsf(lds + kr + 32 * KROW + s * 32);
  SB();
#pragma unroll
  for (int s = 0; s < 8; ++s) s0 = mfma(fa[s], qf[s], s0);
  SB();
  s1 = mfma(fb[0], qf[0], s1);
  SB();
  guard<false>(st, block_max(s0), s0);
  {
    const float mc = -st.m * SOFTMAX_C;
    float ls = st.l;
    SB();
#pragma unroll
    for (int s = 1; s < 8; ++s) {
      s1 = mfma(fb[s], qf[s], s1);
      EXP2(s0, 2 * (s - 1));
      if (s > 1) SUM2(s0, 2 * (s - 2));
      PIN2(s0, 2 * (s - 1));
      SB();
    }
    EXP2(s0, 14); SUM2(s0, 12); SUM2(s0, 14);
    st.l = ls;
    SB();
  }
  guard<true>(st, block_max(s1), s0);
  const float mc = -st.m * SOFTMAX_C;
  float ls = st.l;
  bf16x8 p0 = pack8(s0, 0), p1 = pack8(s0, 8);
  EXP2(s1, 0); PIN2(s1, 0);
  asm volatile("" : "+v"(p0), "+v"(p1));
  SB(); __syncthreads(); SB();
#pragma unroll
  for (int s = 0; s < 8; ++s) fa[s] = ldsf(lds + vr + s * 1024);
#pragma unroll
  for (int s = 0; s < 8; ++s) fb[s] = ldsf(lds + vr + (8 + s) * 1024);
  EXP2(s1, 2); SUM2(s1, 0); PIN2(s1, 2);
  SB();
  bf16x8 p2;
#pragma unroll
  for (int i = 0; i < 8; ++i) {
    st.o[i & 3] = mfma(fa[i], i < 4 ? p0 : p1, st.o[i & 3]);
    if (i < 6) { EXP2(s1, 2 * i + 4); SUM2(s1, 2 * i + 2); PIN2(s1, 2 * i + 4); }
    if (i == 6) { p2 = pack8(s1, 0); SUM2(s1, 14); asm volatile("" : "+v"(p2), "+v"(ls)); }
    SB();
  }
  st.l = ls;
  st.o[0] = mfma(fb[0], p2, st.o[0]);
  const bf16x8 p3 = pack8(s1, 8);
  SB();
#pragma unroll
  for (int i = 1; i < 8; ++i) st.o[i & 3] = mfma(fb[i], i < 4 ? p2 : p3, st.o[i & 3]);
}

__device__ __forceinline__ float merge4(float a, float wa, float b, float wb, float c, float wc, float d, float wd) {
#pragma clang fp contract(off)
  const float p = a * wa, q = b * wb, r = c * wc, s = d * wd;
  return (p + q) + (r + s);
}
struct Wts { float o, p, a, b; };
__device__ __forceinline__ Wts merge_weights(f32x2 mo, f32x2 mp, f32x2 ma, f32x2 mb) {
  const float M = fmaxf(fmaxf(mo[0], mp[0]), fmaxf(ma[0], mb[0]));
  Wts w;
  w.o = __builtin_amdgcn_exp2f((mo[0] - M) * SOFTMAX_C) * mo[1]; w.p = __builtin_amdgcn_exp2f((mp[0] - M) * SOFTMAX_C) * mp[1];
  w.a = __builtin_amdgcn_exp2f((ma[0] - M) * SOFTMAX_C) * ma[1]; w.b = __builtin_amdgcn_exp2f((mb[0] - M) * SOFTMAX_C) * mb[1];
  asm volatile("" : "+v"(w.o), "+v"(w.p), "+v"(w.a), "+v"(w.b));
  const float inv = 1.f / ((w.o + w.p) + (w.a + w.b));
  w.o *= inv; w.p *= inv; w.a *= inv; w.b *= inv;
  return w;
}
__device__ __forceinline__ u32x4 merge_piece(u32x4 xo, u32x4 xp, u32x4 xa, u32x4 xb, const Wts& w) {
  u32x4 r;
#pragma unroll
  for (int j = 0; j < 4; ++j)
    r[j] = pk_bf16(merge4(bf_lo(xo[j]), w.o, bf_lo(xp[j]), w.p, bf_lo(xa[j]), w.a, bf_lo(xb[j]), w.b),
                   merge4(bf_hi(xo[j]), w.o, bf_hi(xp[j]), w.p, bf_hi(xa[j]), w.a, bf_hi(xb[j]), w.b));
  return r;
}
__device__ __forceinline__ f32x2 ld_ml(rsrc_t rs, int lane, int rec) {
  return __builtin_bit_cast(f32x2, __builtin_amdgcn_raw_buffer_load_b64(rs, lane * 8 + PART_ML, rec, AUX_SC1));
}
__device__ __forceinline__ u32x4 ld_piece(rsrc_t rs, int lane, int e, int rec) {
  return __builtin_amdgcn_raw_buffer_load_b128(rs, lane * 16 + e * 1024, rec, AUX_SC1);
}

__global__ void __launch_bounds__(512, 2)
attn_kernel(const __hip_bfloat16* __restrict__ q, const int* __restrict__ qk, const int* __restrict__ qv,
            const __hip_bfloat16* __restrict__ ksc, const __hip_bfloat16* __restrict__ kzr,
            const __hip_bfloat16* __restrict__ vsc, const __hip_bfloat16* __restrict__ vzr,
            __hip_bfloat16* __restrict__ out, char* ws) {
  extern __shared__ __attribute__((aligned(16))) char lds[];
  const int tid = threadIdx.x, lane = tid & 63;
  const int wave = __builtin_amdgcn_readfirstlane(tid >> 6);
  const int bh = blockIdx.x & (NBH - 1), split = blockIdx.x >> 6;
  const int c = lane & 31, h = lane >> 5;

  if (wave < 4) {
    __builtin_amdgcn_s_setprio(3);
    ProdCtx pc;
    const size_t base = ((size_t)bh * SKV + (size_t)split * KPS) * (DH * 4);
    pc.kp = (const char*)qk + base;
    pc.vp = (const char*)qv + base;
    pc.koff = wave * 8192 + lane * 16;
    pc.voff = wave * 8192 + h * 4096 + c * 16;
    pc.lds = lds;
    pc.kw = L_K + (16 * wave + h) * KROW + c * 8;
    pc.vw = L_V + wave * 4096 + lane * 16;
    pc.ksr = c * 8;
    pc.vsr = (16 * wave + 8 * h) * 2;
    Raw r0, r1;
    __syncthreads();
    issue_k(r0, pc.kp, pc.koff); SB();
    issue_v(r0, pc.vp, pc.voff); SB();
    issue_k(r1, pc.kp + TILE * DH * 4, pc.koff); SB();
    __syncthreads();
    SB();
    issue_v(r1, pc.vp + TILE * DH * 4, pc.voff); SB();
    for (int k = 0; k < TPS - 2; k += 2) {
      produce<true>(pc, r0, k);
      produce<true>(pc, r1, k + 1);
    }
    produce<false>(pc, r0, TPS - 2);
    produce<false>(pc, r1, TPS - 1);
  } else {
    const int n = wave - 4;
    const int ct = tid - 256;
    STAMP(0);
    gu64* const ew = (gu64*)(ws + WS_CTL + (bh * 4 + n) * 128);
    gu64* const cw = ew + 8;
    const size_t kso = ((size_t)bh * NGK + (size_t)split * TPS) * DH * 2;
    const size_t vso = ((size_t)bh * SKV + (size_t)split * KPS) * 2;
    u32x4 a0 = ld16<KVQ_SNT>((const char*)ksc + kso + ct * 16), b0 = ld16<KVQ_SNT>((const char*)kzr + kso + ct * 16);
    u32x4 c0 = ld16<KVQ_SNT>((const char*)vsc + vso + ct * 16), d0 = ld16<KVQ_SNT>((const char*)vzr + vso + ct * 16);
    u32x4 a1 = ld16<KVQ_SNT>((const char*)ksc + kso + 4096 + ct * 16), b1 = ld16<KVQ_SNT>((const char*)kzr + kso + 4096 + ct * 16);
    SB();
    bf16x8 qf[8];
    {
      const char* qp = (const char*)q + (((size_t)bh * 4 + n) * SQ + c) * (DH * 2) + h * 16;
#pragma unroll
      for (int s = 0; s < 8; ++s) qf[s] = __builtin_bit_cast(bf16x8, ld16<KVQ_QNT>(qp + s * 32));
    }
    SB();
    u64 e0v = __hip_atomic_load(ew, RLX_AGENT);
    u64 c0v = __hip_atomic_load(cw, RLX_AGENT);
    SB();
    __syncthreads();
    SB();
    *(u32x4*)(lds + L_KS + ct * 16) = a0; *(u32x4*)(lds + L_KZ + ct * 16) = b0;
    *(u32x4*)(lds + L_VS + ct * 16) = c0; *(u32x4*)(lds + L_VZ + ct * 16) = d0;
    *(u32x4*)(lds + L_KS + 4096 + ct * 16) = a1; *(u32x4*)(lds + L_KZ + 4096 + ct * 16) = b1;
    __syncthreads();
    STAMP(1);
    asm volatile("" : "+v"(e0v), "+v"(c0v));
    const u64 epoch = uniform64(e0v);
    const u64 tag = ((epoch + 1) * 0x9E3779B97F4A7C15ull) & ~15ull;
    {
      u64 cur = uniform64(c0v);
      const bool mine = (cur & ~15ull) == tag && (cur & 15ull) != 15ull;
      if (!mine && lane == 0) __hip_atomic_compare_exchange_strong(cw, &cur, tag, __ATOMIC_RELAXED, RLX_AGENT);
    }

    const int pr = (c & 0x13) | ((c & 4) << 1) | ((c & 8) >> 1);
    const unsigned kr = L_K + pr * KROW + h * 16;
    const unsigned vr = L_V + lane * 16;
    ConsState st;
#pragma unroll
    for (int i = 0; i < 4; ++i)
#pragma unroll
      for (int g = 0; g < 16; ++g) st.o[i][g] = 0.f;
    st.m = -__builtin_inff(); st.l = 0.f;
    STAMP(2);
    for (int k = 0; k < TPS; ++k) consume(st, lds, qf, kr, vr);
    STAMP(3);

    const f32x2 ml = {st.m, xhalf_sum(st.l)};
    u32x4 own[8];
    {
      const float il = 1.f / ml[1];
#pragma unroll
      for (int e = 0; e < 8; ++e)
#pragma unroll
        for (int b = 0; b < 2; ++b) {
          const int g = e * 2 + b;
          own[e][2 * b] = pk_bf16(st.o[0][g] * il, st.o[1][g] * il);
          own[e][2 * b + 1] = pk_bf16(st.o[2][g] * il, st.o[3][g] * il);
        }
    }
    const rsrc_t rs = __builtin_amdgcn_make_buffer_rsrc(ws, 0, WS_BYTES, 0x00020000);
    const bool owner = split == n;
    bool ready = false;
    if (owner) {
#pragma unroll
      for (int e = 0; e < 8; ++e) *(u32x4*)(lds + L_OWN + e * 1024 + lane * 16) = own[e];
      *(f32x2*)(lds + L_OWN + PART_ML + lane * 8) = ml;
      for (int spin = 0; spin < KVQ_SPIN; ++spin) {
        if ((unsigned)__builtin_amdgcn_readfirstlane((unsigned)__hip_atomic_load(cw, RLX_AGENT)) == ((unsigned)tag | 3u)) { ready = true; break; }
        __builtin_amdgcn_s_sleep(1);
      }
      if (lane == 0) *(int*)(lds + L_OWN + PART_BYTES) = ready;
    }
    STAMP(4);
    if (!ready) {
      const int rec0 = WS_PART + (bh * 4 + n) * (NSPLIT * PART_BYTES);
      const int rec = rec0 + split * PART_BYTES;
#pragma unroll
      for (int e = 0; e < 8; ++e) __builtin_amdgcn_raw_buffer_store_b128(own[e], rs, lane * 16 + e * 1024, rec, AUX_SC1);
      __builtin_amdgcn_raw_buffer_store_b64(__builtin_bit_cast(u32x2, ml), rs, lane * 8 + PART_ML, rec, AUX_SC1);
      asm volatile("s_waitcnt vmcnt(0)" ::: "memory");
      u64 old = 0;
      if (lane == 0) old = __hip_atomic_fetch_add(cw, owner ? 8ull : 1ull, RLX_AGENT);
      const unsigned o4 = __builtin_amdgcn_readfirstlane((unsigned)old) & 15u;
      if (__builtin_expect(o4 == (owner ? 3u : 10u), 0)) {
        __builtin_amdgcn_fence(__ATOMIC_ACQUIRE, "agent");
        const int rp = rec0 + (split ^ 1) * PART_BYTES, ra = rec0 + ((split & 2) ^ 2) * PART_BYTES, rb = ra + PART_BYTES;
        const Wts w = merge_weights(ml, ld_ml(rs, lane, rp), ld_ml(rs, lane, ra), ld_ml(rs, lane, rb));
        char* op = (char*)out + (((size_t)bh * 4 + n) * SQ + c) * (DH * 2) + h * 32;
#pragma unroll
        for (int e = 0; e < 8; ++e)
          *(u32x4*)(op + (e >> 1) * 64 + (e & 1) * 16) = merge_piece(own[e], ld_piece(rs, lane, e, rp), ld_piece(rs, lane, e, ra), ld_piece(rs, lane, e, rb), w);
        if (lane == 0) { __hip_atomic_store(cw, tag | 15ull, RLX_AGENT); __hip_atomic_store(ew, epoch + 1, RLX_AGENT); }
      }
    }
    __syncthreads();
    STAMP(5);
    if (__builtin_amdgcn_readfirstlane(*(const int*)(lds + L_OWN + PART_BYTES))) {
      const int rec0 = WS_PART + (bh * 4 + split) * (NSPLIT * PART_BYTES);
      const int rp = rec0 + (split ^ 1) * PART_BYTES, ra = rec0 + ((split & 2) ^ 2) * PART_BYTES, rb = ra + PART_BYTES;
      const f32x2 mp = ld_ml(rs, lane, rp), ma = ld_ml(rs, lane, ra), mb = ld_ml(rs, lane, rb);
      const int e0 = 2 * n;
      const u32x4 xp0 = ld_piece(rs, lane, e0, rp), xa0 = ld_piece(rs, lane, e0, ra), xb0 = ld_piece(rs, lane, e0, rb);
      const u32x4 xp1 = ld_piece(rs, lane, e0 + 1, rp), xa1 = ld_piece(rs, lane, e0 + 1, ra), xb1 = ld_piece(rs, lane, e0 + 1, rb);
      const f32x2 mo = *(const f32x2*)(lds + L_OWN + PART_ML + lane * 8);
      const u32x4 xo0 = *(const u32x4*)(lds + L_OWN + e0 * 1024 + lane * 16), xo1 = *(const u32x4*)(lds + L_OWN + e0 * 1024 + 1024 + lane * 16);
      SB();
      const Wts w = merge_weights(mo, mp, ma, mb);
      char* op = (char*)out + (((size_t)bh * 4 + split) * SQ + c) * (DH * 2) + h * 32 + n * 64;
      *(u32x4*)op = merge_piece(xo0, xp0, xa0, xb0, w);
      *(u32x4*)(op + 16) = merge_piece(xo1, xp1, xa1, xb1, w);
      if (owner && lane == 0) { __hip_atomic_store(cw, tag | 15ull, RLX_AGENT); __hip_atomic_store(ew, epoch + 1, RLX_AGENT); }
      STAMP(6);
      STAMP(7);
    }
  }
}

}

extern "C" void kernel_launch(void* const* d_in, const int* in_sizes, int n_in, void* d_out, int out_size,
                              void* d_ws, size_t ws_size, hipStream_t stream) {
  using namespace kvq;
  (void)in_sizes; (void)n_in; (void)out_size; (void)ws_size;
  (void)hipFuncSetAttribute((const void*)attn_kernel, hipFuncAttributeMaxDynamicSharedMemorySize, LDS_BYTES);
  hipLaunchKernelGGL(attn_kernel, dim3(NBH * NSPLIT), dim3(512), LDS_BYTES, stream,
                     (const __hip_bfloat16*)d_in[0], (const int*)d_in[1], (const int*)d_in[2],
                     (const __hip_bfloat16*)d_in[3], (const __hip_bfloat16*)d_in[4],
                     (const __hip_bfloat16*)d_in[5], (const __hip_bfloat16*)d_in[6],
                     (__hip_bfloat16*)d_out, (char*)d_ws);
}
```

```cpp
#define KVQ_SREG 1
#define KVQ_BAR0 1
#define KVQ_W 8
#include <hip/hip_runtime.h>
#include <hip/hip_bf16.h>
#include <stdint.h>

namespace kvq {

constexpr int NB = 8, HQ = 32, HKV = 8, SQ = 32, SKV = 8192, DH = 128;
constexpr int TILE = 64;
constexpr int NGK = SKV / TILE;
constexpr int NSPLIT = 4;
constexpr int NBH = NB * HKV;
constexpr int TPS = NGK / NSPLIT;
constexpr int KPS = TPS * TILE;
#define KVQ_QNT 1
#define KVQ_SNT 1
#define KVQ_DRY 0
#define KVQ_SPIN 4096
#define KVQ_ACQ 0
constexpr int DEPTH = 2;

constexpr int KROW = 272;
constexpr int L_K = 0;
constexpr int L_V = TILE * KROW;
constexpr int L_KS = L_V + 16 * 1024;
constexpr int L_KZ = L_KS + TPS * DH * 2;
constexpr int L_VS = L_KZ + TPS * DH * 2;
constexpr int L_VZ = L_VS + KPS * 2;
constexpr int L_OWN = L_VZ + KPS * 2;
constexpr int L_Q = L_OWN + 8 * 1024 + 512 + 16;
constexpr int LDS_BYTES = L_Q + 4 * SQ * KROW;

constexpr int WS_CTL = 0;
constexpr int WS_PART = NBH * 4 * 128;
constexpr int PART_ML = 8 * 1024;
constexpr int PART_BYTES = PART_ML + 512;
constexpr int WS_BYTES = WS_PART + NSPLIT * NBH * 4 * PART_BYTES;

constexpr float SOFTMAX_C = 0.08838834764831845f * 1.4426950408889634f;

typedef int i32x4 __attribute__((ext_vector_type(4)));
typedef unsigned u32x4 __attribute__((ext_vector_type(4)));
typedef unsigned u32x2 __attribute__((ext_vector_type(2)));
typedef float f32x2 __attribute__((ext_vector_type(2)));
typedef float f32x16 __attribute__((ext_vector_type(16)));
typedef __bf16 bf16x8 __attribute__((ext_vector_type(8)));
typedef __bf16 bf16x2 __attribute__((ext_vector_type(2)));
typedef unsigned long long u64;
typedef __attribute__((address_space(1))) u64 gu64;
typedef __amdgpu_buffer_rsrc_t rsrc_t;
#define RLX_AGENT __ATOMIC_RELAXED, __HIP_MEMORY_SCOPE_AGENT
#define SB() __builtin_amdgcn_sched_barrier(0)
constexpr int AUX_SC1 = 16, AUX_NT = 2;

#define STAMP(i) do {} while (0)

__device__ __forceinline__ float bf_lo(unsigned w) { return __uint_as_float(w << 16); }
__device__ __forceinline__ float bf_hi(unsigned w) { return __uint_as_float(w & 0xffff0000u); }
__device__ __forceinline__ unsigned pk_bf16(float a, float b) {
  f32x2 v = {a, b};
  return __builtin_bit_cast(unsigned, __builtin_convertvector(v, bf16x2));
}
__device__ __forceinline__ float xhalf_max(float x) {
  auto r = __builtin_amdgcn_permlane32_swap(__float_as_uint(x), __float_as_uint(x), false, false);
  return fmaxf(__uint_as_float(r[0]), __uint_as_float(r[1]));
}
__device__ __forceinline__ float xhalf_sum(float x) {
  auto r = __builtin_amdgcn_permlane32_swap(__float_as_uint(x), __float_as_uint(x), false, false);
  return __uint_as_float(r[0]) + __uint_as_float(r[1]);
}
template <int NT> __device__ __forceinline__ u32x4 ld16(const void* p) {
  if constexpr (NT) return __builtin_nontemporal_load((const u32x4*)p); else return *(const u32x4*)p;
}
__device__ __forceinline__ u64 uniform64(u64 x) {
  const unsigned lo = __builtin_amdgcn_readfirstlane((unsigned)x), hi = __builtin_amdgcn_readfirstlane((unsigned)(x >> 32));
  return ((u64)hi << 32) | lo;
}

struct Raw { u32x4 k[8]; u32x4 v[8]; };

struct ProdCtx {
  rsrc_t rk, rv;
  unsigned koff, voff;
  char* lds;
  unsigned kw, vw;
  unsigned ksr, vsr;
};
__device__ __forceinline__ u32x4 ld_k(const ProdCtx& c, int t, int i) {
  return __builtin_amdgcn_raw_buffer_load_b128(c.rk, c.koff + t * (TILE * DH * 4) + (i & 3) * 1024, (i >> 2) * 4096, AUX_NT);
}
__device__ __forceinline__ u32x4 ld_v(const ProdCtx& c, int t, int j) {
  return __builtin_amdgcn_raw_buffer_load_b128(c.rv, c.voff + t * (TILE * DH * 4) + j * 512, 0, AUX_NT);
}
__device__ __forceinline__ float i2f(unsigned x) { return (float)(int)x; }

struct Scales { u32x2 ks, kz; u32x4 vs, vz; };
__device__ __forceinline__ void produce(const ProdCtx& c, u32x4 (&rk)[8], u32x4 (&rv)[8], Scales& sc, int k) {
  constexpr int D = KVQ_W / 16;
  {
    const u32x2 sw = sc.ks, zw = sc.kz;
    const float s0 = bf_lo(sw[0]), s1 = bf_hi(sw[0]), s2 = bf_lo(sw[1]), s3 = bf_hi(sw[1]);
    const float c0 = -bf_lo(zw[0]) * s0, c1 = -bf_hi(zw[0]) * s1, c2 = -bf_lo(zw[1]) * s2, c3 = -bf_hi(zw[1]) * s3;
    SB();
#pragma unroll
    for (int t = 0; t < 8; ++t) {
      const u32x4 x = rk[t];
      u32x2 w;
      w[0] = pk_bf16(fmaf(i2f(x[0]), s0, c0), fmaf(i2f(x[1]), s1, c1));
      w[1] = pk_bf16(fmaf(i2f(x[2]), s2, c2), fmaf(i2f(x[3]), s3, c3));
      asm volatile("" : "+v"(w));
      SB();
      if (KVQ_W == 8) rv[t] = ld_v(c, k, t); else rk[t] = ld_k(c, k + D, t);
      *(u32x2*)(c.lds + c.kw + t * (2 * KROW)) = w;
      SB();
    }
  }
  const u32x4 sw = sc.vs, zw = sc.vz;
  SB(); __syncthreads(); SB();
  sc.ks = *(const u32x2*)(c.lds + L_KS + c.ksr + (k + 1) * (DH * 2));
  sc.kz = *(const u32x2*)(c.lds + L_KZ + c.ksr + (k + 1) * (DH * 2));
  sc.vs = *(const u32x4*)(c.lds + L_VS + c.vsr + (k + 1) * (TILE * 2));
  sc.vz = *(const u32x4*)(c.lds + L_VZ + c.vsr + (k + 1) * (TILE * 2));
  SB();
  {
    u32x4 f0, f1, f2, f3;
#pragma unroll
    for (int jj = 0; jj < 4; ++jj) {
      const float sa = bf_lo(sw[jj]), sb = bf_hi(sw[jj]);
      const float ca = -bf_lo(zw[jj]) * sa, cb = -bf_hi(zw[jj]) * sb;
      const u32x4 xa = rv[2 * jj], xb = rv[2 * jj + 1];
      unsigned g0 = pk_bf16(fmaf(i2f(xa[0]), sa, ca), fmaf(i2f(xb[0]), sb, cb));
      unsigned g1 = pk_bf16(fmaf(i2f(xa[1]), sa, ca), fmaf(i2f(xb[1]), sb, cb));
      unsigned g2 = pk_bf16(fmaf(i2f(xa[2]), sa, ca), fmaf(i2f(xb[2]), sb, cb));
      unsigned g3 = pk_bf16(fmaf(i2f(xa[3]), sa, ca), fmaf(i2f(xb[3]), sb, cb));
      asm volatile("" : "+v"(g0), "+v"(g1), "+v"(g2), "+v"(g3));
      f0[jj] = g0; f1[jj] = g1; f2[jj] = g2; f3[jj] = g3;
      SB();
      if (KVQ_W == 8) { rk[2 * jj] = ld_k(c, k + 1, 2 * jj); rk[2 * jj + 1] = ld_k(c, k + 1, 2 * jj + 1); }
      else { rv[2 * jj] = ld_v(c, k + D, 2 * jj); rv[2 * jj + 1] = ld_v(c, k + D, 2 * jj + 1); }
      SB();
    }
    char* vb = c.lds + c.vw;
    *(u32x4*)(vb + 0 * 1024) = f0;
    *(u32x4*)(vb + 1 * 1024) = f1;
    *(u32x4*)(vb + 2 * 1024) = f2;
    *(u32x4*)(vb + 3 * 1024) = f3;
  }
  SB(); __syncthreads(); SB();
}

struct ConsState { f32x16 o[4]; float m, l; };

constexpr float RESC_THR = 8.0f;
__device__ __forceinline__ float block_max(const f32x16& s) {
  float a = fmaxf(fmaxf(s[0], s[1]), s[2]), b = fmaxf(fmaxf(s[3], s[4]), s[5]);
  a = fmaxf(fmaxf(a, s[6]), s[7]); b = fmaxf(fmaxf(b, s[8]), s[9]);
  a = fmaxf(fmaxf(a, s[10]), s[11]); b = fmaxf(fmaxf(b, s[12]), s[13]);
  a = fmaxf(fmaxf(a, s[14]), s[15]);
  return xhalf_max(fmaxf(a, b)) * SOFTMAX_C;
}
template <bool PEND>
__device__ __forceinline__ void guard(ConsState& st, float mx, f32x16& pend) {
  if (__builtin_expect(__builtin_amdgcn_ballot_w64(!(mx - st.m <= RESC_THR)) != 0ull, 0)) {
    const float mn = fmaxf(st.m, mx);
    const float alpha = __builtin_amdgcn_exp2f(st.m - mn);
    st.m = mn;
    st.l *= alpha;
#pragma unroll
    for (int i = 0; i < 4; ++i)
#pragma unroll
      for (int g = 0; g < 16; ++g) st.o[i][g] *= alpha;
    if (PEND) {
#pragma unroll
      for (int g = 0; g < 16; ++g) pend[g] *= alpha;
    }
  }
}
__device__ __forceinline__ bf16x8 ldsf(const char* p) { return *(const bf16x8*)p; }
__device__ __forceinline__ f32x16 mfma(bf16x8 a, bf16x8 b, f32x16 c) { return __builtin_amdgcn_mfma_f32_32x32x16_bf16(a, b, c, 0, 0, 0); }
__device__ __forceinline__ bf16x8 pack8(const f32x16& p, int o) {
  u32x4 w = {pk_bf16(p[o], p[o + 1]), pk_bf16(p[o + 2], p[o + 3]), pk_bf16(p[o + 4], p[o + 5]), pk_bf16(p[o + 6], p[o + 7])};
  return __builtin_bit_cast(bf16x8, w);
}
#define EXP2(S, g) do { S[g] = __builtin_amdgcn_exp2f(fmaf(S[g], SOFTMAX_C, mc)); S[(g) + 1] = __builtin_amdgcn_exp2f(fmaf(S[(g) + 1], SOFTMAX_C, mc)); } while (0)
#define SUM2(S, g) do { ls += S[g]; ls += S[(g) + 1]; } while (0)
#define PIN2(S, g) do { float e0_ = S[g], e1_ = S[(g) + 1]; asm volatile("" : "+v"(e0_), "+v"(e1_), "+v"(ls)); S[g] = e0_; S[(g) + 1] = e1_; } while (0)

__device__ __forceinline__ void consume(ConsState& st, const char* lds, const bf16x8 (&qf)[8], unsigned kr, unsigned vr) {
  f32x16 s0, s1;
#pragma unroll
  for (int g = 0; g < 16; ++g) { s0[g] = 0.f; s1[g] = 0.f; }
  bf16x8 fa[8], fb[8];
  SB(); __syncthreads(); SB();
#pragma unroll
  for (int s = 0; s < 8; ++s) fa[s] = ldsf(lds + kr + s * 32);
#pragma unroll
  for (int s = 0; s < 8; ++s) fb[s] = ldsf(lds + kr + 32 * KROW + s * 32);
  SB();
#pragma unroll
  for (int s = 0; s < 8; ++s) s0 = mfma(fa[s], qf[s], s0);
  SB();
  s1 = mfma(fb[0], qf[0], s1);
  SB();
  guard<false>(st, block_max(s0), s0);
  {
    const float mc = -st.m;
    float ls = st.l;
    SB();
#pragma unroll
    for (int s = 1; s < 8; ++s) {
      s1 = mfma(fb[s], qf[s], s1);
      EXP2(s0, 2 * (s - 1));
      if (s > 1) SUM2(s0, 2 * (s - 2));
      PIN2(s0, 2 * (s - 1));
      SB();
    }
    EXP2(s0, 14); SUM2(s0, 12); SUM2(s0, 14);
    st.l = ls;
    SB();
  }
  guard<true>(st, block_max(s1), s0);
  const float mc = -st.m;
  float ls = st.l;
  bf16x8 p0 = pack8(s0, 0), p1 = pack8(s0, 8);
  EXP2(s1, 0); PIN2(s1, 0);
  asm volatile("" : "+v"(p0), "+v"(p1));
  SB(); __syncthreads(); SB();
#pragma unroll
  for (int s = 0; s < 8; ++s) fa[s] = ldsf(lds + vr + s * 1024);
#pragma unroll
  for (int s = 0; s < 8; ++s) fb[s] = ldsf(lds + vr + (8 + s) * 1024);
  EXP2(s1, 2); SUM2(s1, 0); PIN2(s1, 2);
  SB();
  bf16x8 p2;
#pragma unroll
  for (int i = 0; i < 8; ++i) {
    st.o[i & 3] = mfma(fa[i], i < 4 ? p0 : p1, st.o[i & 3]);
    if (i < 6) { EXP2(s1, 2 * i + 4); SUM2(s1, 2 * i + 2); PIN2(s1, 2 * i + 4); }
    if (i == 6) { p2 = pack8(s1, 0); SUM2(s1, 14); asm volatile("" : "+v"(p2), "+v"(ls)); }
    SB();
  }
  st.l = ls;
  st.o[0] = mfma(fb[0], p2, st.o[0]);
  const bf16x8 p3 = pack8(s1, 8);
  SB();
#pragma unroll
  for (int i = 1; i < 8; ++i) st.o[i & 3] = mfma(fb[i], i < 4 ? p2 : p3, st.o[i & 3]);
}

__device__ __forceinline__ float merge4(float a, float wa, float b, float wb, float c, float wc, float d, float wd) {
#pragma clang fp contract(off)
  const float p = a * wa, q = b * wb, r = c * wc, s = d * wd;
  return (p + q) + (r + s);
}
struct Wts { float o, p, a, b; };
__device__ __forceinline__ Wts merge_weights(f32x2 mo, f32x2 mp, f32x2 ma, f32x2 mb) {
  const float M = fmaxf(fmaxf(mo[0], mp[0]), fmaxf(ma[0], mb[0]));
  Wts w;
  w.o = __builtin_amdgcn_exp2f(mo[0] - M) * mo[1]; w.p = __builtin_amdgcn_exp2f(mp[0] - M) * mp[1];
  w.a = __builtin_amdgcn_exp2f(ma[0] - M) * ma[1]; w.b = __builtin_amdgcn_exp2f(mb[0] - M) * mb[1];
  asm volatile("" : "+v"(w.o), "+v"(w.p), "+v"(w.a), "+v"(w.b));
  const float inv = 1.f / ((w.o + w.p) + (w.a + w.b));
  w.o *= inv; w.p *= inv; w.a *= inv; w.b *= inv;
  return w;
}
__device__ __forceinline__ u32x4 merge_piece(u32x4 xo, u32x4 xp, u32x4 xa, u32x4 xb, const Wts& w) {
  u32x4 r;
#pragma unroll
  for (int j = 0; j < 4; ++j)
    r[j] = pk_bf16(merge4(bf_lo(xo[j]), w.o, bf_lo(xp[j]), w.p, bf_lo(xa[j]), w.a, bf_lo(xb[j]), w.b),
                   merge4(bf_hi(xo[j]), w.o, bf_hi(xp[j]), w.p, bf_hi(xa[j]), w.a, bf_hi(xb[j]), w.b));
  return r;
}
__device__ __forceinline__ f32x2 ld_ml(rsrc_t rs, int lane, int rec) {
  return __builtin_bit_cast(f32x2, __builtin_amdgcn_raw_buffer_load_b64(rs, lane * 8 + PART_ML, rec, AUX_SC1));
}
__device__ __forceinline__ u32x4 ld_piece(rsrc_t rs, int lane, int e, int rec) {
  return __builtin_amdgcn_raw_buffer_load_b128(rs, lane * 16 + e * 1024, rec, AUX_SC1);
}

__global__ void __launch_bounds__(512, 2)
attn_kernel(const __hip_bfloat16* __restrict__ q, const int* __restrict__ qk, const int* __restrict__ qv,
            const __hip_bfloat16* __restrict__ ksc, const __hip_bfloat16* __restrict__ kzr,
            const __hip_bfloat16* __restrict__ vsc, const __hip_bfloat16* __restrict__ vzr,
            __hip_bfloat16* __restrict__ out, char* ws) {
  extern __shared__ __attribute__((aligned(16))) char lds[];
  const int tid = threadIdx.x, lane = tid & 63;
  const int wave = __builtin_amdgcn_readfirstlane(tid >> 6);
  const int bh = blockIdx.x & (NBH - 1), split = blockIdx.x >> 6;
  const int c = lane & 31, h = lane >> 5;

  if (wave < 4) {
    __builtin_amdgcn_s_setprio(3);
    ProdCtx pc;
    const size_t base = ((size_t)bh * SKV + (size_t)split * KPS) * (DH * 4);
    pc.rk = __builtin_amdgcn_make_buffer_rsrc((char*)qk + base, 0, KPS * DH * 4, 0x00020000);
    pc.rv = __builtin_amdgcn_make_buffer_rsrc((char*)qv + base, 0, KPS * DH * 4, 0x00020000);
    pc.koff = wave * 8192 + lane * 16;
    pc.voff = wave * 8192 + h * 4096 + c * 16;
    pc.lds = lds;
    pc.kw = L_K + (16 * wave + h) * KROW + c * 8;
    pc.vw = L_V + wave * 4096 + lane * 16;
    pc.ksr = c * 8;
    pc.vsr = (16 * wave + 8 * h) * 2;
    Raw r0, r1;
    Scales sc;
    __syncthreads();
    {
      const size_t kso = ((size_t)bh * NGK + (size_t)split * TPS) * DH * 2 + pc.ksr;
      const size_t vso = ((size_t)bh * SKV + (size_t)split * KPS) * 2 + pc.vsr;
      sc.ks = __builtin_nontemporal_load((const u32x2*)((const char*)ksc + kso));
      sc.kz = __builtin_nontemporal_load((const u32x2*)((const char*)kzr + kso));
      sc.vs = __builtin_nontemporal_load((const u32x4*)((const char*)vsc + vso));
      sc.vz = __builtin_nontemporal_load((const u32x4*)((const char*)vzr + vso));
    }
    SB();
#pragma unroll
    for (int i = 0; i < 8; ++i) r0.k[i] = ld_k(pc, 0, i);
    SB();
    if (KVQ_W >= 16) {
#pragma unroll
      for (int i = 0; i < 8; ++i) r0.v[i] = ld_v(pc, 0, i);
      SB();
    }
    if (KVQ_W >= 32) {
#pragma unroll
      for (int i = 0; i < 8; ++i) r1.k[i] = ld_k(pc, 1, i);
      SB();
#pragma unroll
      for (int i = 0; i < 8; ++i) r1.v[i] = ld_v(pc, 1, i);
      SB();
    }
    SB();
    if (KVQ_W >= 32) {
      for (int k = 0; k < TPS; k += 2) {
        produce(pc, r0.k, r0.v, sc, k);
        produce(pc, r1.k, r1.v, sc, k + 1);
      }
    } else {
      for (int k = 0; k < TPS; ++k) produce(pc, r0.k, r0.v, sc, k);
    }
    asm volatile("s_waitcnt vmcnt(0)" ::: "memory");
  } else {
    const int n = wave - 4;
    const int ct = tid - 256;
    STAMP(0);
    gu64* const ew = (gu64*)(ws + WS_CTL + (bh * 4 + n) * 128);
    gu64* const cw = ew + 8;
    const size_t kso = ((size_t)bh * NGK + (size_t)split * TPS) * DH * 2;
    const size_t vso = ((size_t)bh * SKV + (size_t)split * KPS) * 2;
    u32x4 a0 = ld16<KVQ_SNT>((const char*)ksc + kso + ct * 16), b0 = ld16<KVQ_SNT>((const char*)kzr + kso + ct * 16);
    u32x4 c0 = ld16<KVQ_SNT>((const char*)vsc + vso + ct * 16), d0 = ld16<KVQ_SNT>((const char*)vzr + vso + ct * 16);
    u32x4 a1 = ld16<KVQ_SNT>((const char*)ksc + kso + 4096 + ct * 16), b1 = ld16<KVQ_SNT>((const char*)kzr + kso + 4096 + ct * 16);
    SB();
    u32x4 qraw[8];
    {
      const char* qp = (const char*)q + ((size_t)bh * 4 + n) * (SQ * DH * 2) + lane * 16;
#pragma unroll
      for (int i = 0; i < 8; ++i) qraw[i] = ld16<KVQ_QNT>(qp + i * 1024);
    }
    SB();
    u64 e0v = __hip_atomic_load(ew, RLX_AGENT);
    u64 c0v = __hip_atomic_load(cw, RLX_AGENT);
    SB();
    __syncthreads();
    SB();
    *(u32x4*)(lds + L_KS + ct * 16) = a0; *(u32x4*)(lds + L_KZ + ct * 16) = b0;
    *(u32x4*)(lds + L_VS + ct * 16) = c0; *(u32x4*)(lds + L_VZ + ct * 16) = d0;
    *(u32x4*)(lds + L_KS + 4096 + ct * 16) = a1; *(u32x4*)(lds + L_KZ + 4096 + ct * 16) = b1;
    STAMP(1);
    bf16x8 qf[8];
    {
      char* qs = lds + L_Q + n * (SQ * KROW);
#pragma unroll
      for (int i = 0; i < 8; ++i) *(u32x4*)(qs + (4 * i + (lane >> 4)) * KROW + (lane & 15) * 16) = qraw[i];
      __builtin_amdgcn_fence(__ATOMIC_RELEASE, "wavefront"); __builtin_amdgcn_wave_barrier(); __builtin_amdgcn_fence(__ATOMIC_ACQUIRE, "wavefront");
#pragma unroll
      for (int s = 0; s < 8; ++s) qf[s] = *(const bf16x8*)(qs + c * KROW + h * 16 + s * 32);
    }
    asm volatile("" : "+v"(e0v), "+v"(c0v));
    const u64 epoch = uniform64(e0v);
    const u64 tag = ((epoch + 1) * 0x9E3779B97F4A7C15ull) & ~15ull;
    const u64 cfound = uniform64(c0v);

    const int pr = (c & 0x13) | ((c & 4) << 1) | ((c & 8) >> 1);
    const unsigned kr = L_K + pr * KROW + h * 16;
    const unsigned vr = L_V + lane * 16;
    ConsState st;
#pragma unroll
    for (int i = 0; i < 4; ++i)
#pragma unroll
      for (int g = 0; g < 16; ++g) st.o[i][g] = 0.f;
    st.m = -__builtin_inff(); st.l = 0.f;
    STAMP(2);
    constexpr bool real = true;
    {
    {
      u64 cur = cfound;
      const bool mine = (cur & ~15ull) == tag && (cur & 15ull) != 15ull;
      if (!mine && lane == 0) __hip_atomic_compare_exchange_strong(cw, &cur, tag, __ATOMIC_RELAXED, RLX_AGENT);
      for (int k = 0; k < TPS; ++k) consume(st, lds, qf, kr, vr);
    }
    STAMP(3);

    const f32x2 ml = {st.m, xhalf_sum(st.l)};
    u32x4 own[8];
    {
      const float il = 1.f / ml[1];
#pragma unroll
      for (int e = 0; e < 8; ++e)
#pragma unroll
        for (int b = 0; b < 2; ++b) {
          const int g = e * 2 + b;
          own[e][2 * b] = pk_bf16(st.o[0][g] * il, st.o[1][g] * il);
          own[e][2 * b + 1] = pk_bf16(st.o[2][g] * il, st.o[3][g] * il);
        }
    }
    const rsrc_t rs = __builtin_amdgcn_make_buffer_rsrc(ws, 0, real ? WS_BYTES : 0, 0x00020000);
    const rsrc_t ro = __builtin_amdgcn_make_buffer_rsrc(out, 0, real ? NB * HQ * SQ * DH * 2 : 0, 0x00020000);
    const int cwo = WS_CTL + (bh * 4 + n) * 128 + 64;
    const bool owner = split == n;
    bool ready = false;
    if (owner) {
#pragma unroll
      for (int e = 0; e < 8; ++e) *(u32x4*)(lds + L_OWN + e * 1024 + lane * 16) = own[e];
      *(f32x2*)(lds + L_OWN + PART_ML + lane * 8) = ml;
      const unsigned want = real ? ((unsigned)tag | 3u) : 0u;
#pragma nounroll
      for (int spin = 0; spin < KVQ_SPIN; ++spin) {
        asm volatile("" ::: "memory");
        if ((unsigned)__builtin_amdgcn_readfirstlane(__builtin_amdgcn_raw_buffer_load_b32(rs, cwo, 0, AUX_SC1)) == want) { ready = true; break; }
        __builtin_amdgcn_s_sleep(1);
      }
      if (lane == 0) *(int*)(lds + L_OWN + PART_BYTES) = ready;
    }
    STAMP(4);
    if (!ready) {
      const int rec0 = WS_PART + (bh * 4 + n) * (NSPLIT * PART_BYTES);
      const int rec = rec0 + split * PART_BYTES;
#pragma unroll
      for (int e = 0; e < 8; ++e) __builtin_amdgcn_raw_buffer_store_b128(own[e], rs, lane * 16 + e * 1024, rec, AUX_SC1);
      __builtin_amdgcn_raw_buffer_store_b64(__builtin_bit_cast(u32x2, ml), rs, lane * 8 + PART_ML, rec, AUX_SC1);
      asm volatile("s_waitcnt vmcnt(0)" ::: "memory");
      const int old = __builtin_amdgcn_raw_ptr_buffer_atomic_add_i32(owner ? 8 : 1, rs, lane == 0 ? cwo : 0x7ffffff0, 0, 0);
      const unsigned o4 = (unsigned)__builtin_amdgcn_readfirstlane(old) & 15u;
      if (__builtin_expect(o4 == (owner ? 3u : 10u), 0)) {
        __builtin_amdgcn_fence(__ATOMIC_ACQUIRE, "agent");
        const int rp = rec0 + (split ^ 1) * PART_BYTES, ra = rec0 + ((split & 2) ^ 2) * PART_BYTES, rb = ra + PART_BYTES;
        const Wts w = merge_weights(ml, ld_ml(rs, lane, rp), ld_ml(rs, lane, ra), ld_ml(rs, lane, rb));
        const int op = ((bh * 4 + n) * SQ + c) * (DH * 2) + h * 32;
#pragma unroll
        for (int e = 0; e < 8; ++e)
          __builtin_amdgcn_raw_buffer_store_b128(merge_piece(own[e], ld_piece(rs, lane, e, rp), ld_piece(rs, lane, e, ra), ld_piece(rs, lane, e, rb), w),
                                                 ro, op + (e >> 1) * 64 + (e & 1) * 16, 0, 0);
        if (lane == 0) { __hip_atomic_store(cw, tag | 15ull, RLX_AGENT); __hip_atomic_store(ew, epoch + 1, RLX_AGENT); }
      }
    }
    if (real) __syncthreads();
    STAMP(5);
    if (!real || __builtin_amdgcn_readfirstlane(*(const int*)(lds + L_OWN + PART_BYTES))) {
      const int rec0 = WS_PART + (bh * 4 + split) * (NSPLIT * PART_BYTES);
      const int rp = rec0 + (split ^ 1) * PART_BYTES, ra = rec0 + ((split & 2) ^ 2) * PART_BYTES, rb = ra + PART_BYTES;
      const f32x2 mp = ld_ml(rs, lane, rp), ma = ld_ml(rs, lane, ra), mb = ld_ml(rs, lane, rb);
      const int e0 = 2 * n;
      const u32x4 xp0 = ld_piece(rs, lane, e0, rp), xa0 = ld_piece(rs, lane, e0, ra), xb0 = ld_piece(rs, lane, e0, rb);
      const u32x4 xp1 = ld_piece(rs, lane, e0 + 1, rp), xa1 = ld_piece(rs, lane, e0 + 1, ra), xb1 = ld_piece(rs, lane, e0 + 1, rb);
      const f32x2 mo = *(const f32x2*)(lds + L_OWN + PART_ML + lane * 8);
      const u32x4 xo0 = *(const u32x4*)(lds + L_OWN + e0 * 1024 + lane * 16), xo1 = *(const u32x4*)(lds + L_OWN + e0 * 1024 + 1024 + lane * 16);
      SB();
      const Wts w = merge_weights(mo, mp, ma, mb);
      const int op = ((bh * 4 + split) * SQ + c) * (DH * 2) + h * 32 + n * 64;
      __builtin_amdgcn_raw_buffer_store_b128(merge_piece(xo0, xp0, xa0, xb0, w), ro, op, 0, 0);
      __builtin_amdgcn_raw_buffer_store_b128(merge_piece(xo1, xp1, xa1, xb1, w), ro, op + 16, 0, 0);
      if (real && owner && lane == 0) { __hip_atomic_store(cw, tag | 15ull, RLX_AGENT); __hip_atomic_store(ew, epoch + 1, RLX_AGENT); }
      STAMP(6);
      STAMP(7);
    }
    }
  }
}

}

extern "C" void kernel_launch(void* const* d_in, const int* in_sizes, int n_in, void* d_out, int out_size,
                              void* d_ws, size_t ws_size, hipStream_t stream) {
  using namespace kvq;
  (void)in_sizes; (void)n_in; (void)out_size; (void)ws_size;
  (void)hipFuncSetAttribute((const void*)attn_kernel, hipFuncAttributeMaxDynamicSharedMemorySize, LDS_BYTES);
  hipLaunchKernelGGL(attn_kernel, dim3(NBH * NSPLIT), dim3(512), LDS_BYTES, stream,
                     (const __hip_bfloat16*)d_in[0], (const int*)d_in[1], (const int*)d_in[2],
                     (const __hip_bfloat16*)d_in[3], (const __hip_bfloat16*)d_in[4],
                     (const __hip_bfloat16*)d_in[5], (const __hip_bfloat16*)d_in[6],
                     (__hip_bfloat16*)d_out, (char*)d_ws);
}
```

```cpp
#include <hip/hip_runtime.h>
#include <hip/hip_bf16.h>
#include <stdint.h>

namespace kvq {

constexpr int NB = 8, HQ = 32, HKV = 8, SQ = 32, SKV = 8192, DH = 128;
constexpr int TILE = 64;
constexpr int NGK = SKV / TILE;
constexpr int NSPLIT = 4;
constexpr int NBH = NB * HKV;
constexpr int TPS = NGK / NSPLIT;
constexpr int KPS = TPS * TILE;
constexpr int SPIN_CAP = 4096;

constexpr int KROW = 272;
constexpr int L_K = 0;
constexpr int L_V = TILE * KROW;
constexpr int L_KS = L_V + 16 * 1024;
constexpr int L_KZ = L_KS + TPS * DH * 2;
constexpr int L_VS = L_KZ + TPS * DH * 2;
constexpr int L_VZ = L_VS + KPS * 2;
constexpr int L_OWN = L_VZ + KPS * 2;
constexpr int L_Q = L_OWN + 8 * 1024 + 512 + 16;
constexpr int LDS_BYTES = L_Q + 4 * SQ * KROW;

constexpr int PART_ML = 8 * 1024;
constexpr int PART_BYTES = PART_ML + 512;
constexpr int GRP_BYTES = 128 + NSPLIT * PART_BYTES;
constexpr int WS_BYTES = NBH * 4 * GRP_BYTES;

constexpr float SOFTMAX_C = 0.08838834764831845f * 1.4426950408889634f;

typedef unsigned u32x4 __attribute__((ext_vector_type(4)));
typedef unsigned u32x2 __attribute__((ext_vector_type(2)));
typedef float f32x2 __attribute__((ext_vector_type(2)));
typedef float f32x16 __attribute__((ext_vector_type(16)));
typedef __bf16 bf16x8 __attribute__((ext_vector_type(8)));
typedef __bf16 bf16x2 __attribute__((ext_vector_type(2)));
typedef unsigned long long u64;
typedef __attribute__((address_space(1))) u64 gu64;
typedef __amdgpu_buffer_rsrc_t rsrc_t;
#define RLX_AGENT __ATOMIC_RELAXED, __HIP_MEMORY_SCOPE_AGENT
#define SB() __builtin_amdgcn_sched_barrier(0)
#define WG_BARRIER() do { SB(); __syncthreads(); SB(); } while (0)
constexpr int AUX_SC1 = 16, AUX_NT = 2;

__device__ __forceinline__ float bf_lo(unsigned w) { return __uint_as_float(w << 16); }
__device__ __forceinline__ float bf_hi(unsigned w) { return __uint_as_float(w & 0xffff0000u); }
__device__ __forceinline__ float i2f(unsigned x) { return (float)(int)x; }
__device__ __forceinline__ unsigned pk_bf16(float a, float b) {
  f32x2 v = {a, b};
  return __builtin_bit_cast(unsigned, __builtin_convertvector(v, bf16x2));
}
__device__ __forceinline__ float xhalf_max(float x) {
  auto r = __builtin_amdgcn_permlane32_swap(__float_as_uint(x), __float_as_uint(x), false, false);
  return fmaxf(__uint_as_float(r[0]), __uint_as_float(r[1]));
}
__device__ __forceinline__ float xhalf_sum(float x) {
  auto r = __builtin_amdgcn_permlane32_swap(__float_as_uint(x), __float_as_uint(x), false, false);
  return __uint_as_float(r[0]) + __uint_as_float(r[1]);
}
template <class T> __device__ __forceinline__ T ld_nt(const void* p) { return __builtin_nontemporal_load((const T*)p); }
__device__ __forceinline__ u64 uniform64(u64 x) {
  const unsigned lo = __builtin_amdgcn_readfirstlane((unsigned)x), hi = __builtin_amdgcn_readfirstlane((unsigned)(x >> 32));
  return ((u64)hi << 32) | lo;
}
__device__ __forceinline__ rsrc_t mk_rsrc(const void* p, int bytes) {
  return __builtin_amdgcn_make_buffer_rsrc((void*)p, 0, bytes, 0x00020000);
}

struct ProdCtx {
  rsrc_t rk, rv;
  unsigned koff, voff;
  char* lds;
  unsigned kw, vw;
  unsigned ksr, vsr;
};
struct Scales { u32x2 ks, kz; u32x4 vs, vz; };

__device__ __forceinline__ u32x4 ld_k(const ProdCtx& c, int t, int i) {
  return __builtin_amdgcn_raw_buffer_load_b128(c.rk, c.koff + t * (TILE * DH * 4) + (i & 3) * 1024, (i >> 2) * 4096, AUX_NT);
}
__device__ __forceinline__ u32x4 ld_v(const ProdCtx& c, int t, int j) {
  return __builtin_amdgcn_raw_buffer_load_b128(c.rv, c.voff + t * (TILE * DH * 4) + j * 512, 0, AUX_NT);
}

__device__ __forceinline__ void produce(const ProdCtx& c, u32x4 (&rk)[8], Scales& sc, int k) {
  u32x4 rv[8];
  {
    const float s0 = bf_lo(sc.ks[0]), s1 = bf_hi(sc.ks[0]), s2 = bf_lo(sc.ks[1]), s3 = bf_hi(sc.ks[1]);
    const float c0 = -bf_lo(sc.kz[0]) * s0, c1 = -bf_hi(sc.kz[0]) * s1, c2 = -bf_lo(sc.kz[1]) * s2, c3 = -bf_hi(sc.kz[1]) * s3;
    SB();
#pragma unroll
    for (int t = 0; t < 8; ++t) {
      const u32x4 x = rk[t];
      u32x2 w;
      w[0] = pk_bf16(fmaf(i2f(x[0]), s0, c0), fmaf(i2f(x[1]), s1, c1));
      w[1] = pk_bf16(fmaf(i2f(x[2]), s2, c2), fmaf(i2f(x[3]), s3, c3));
      asm volatile("" : "+v"(w));
      SB();
      rv[t] = ld_v(c, k, t);
      *(u32x2*)(c.lds + c.kw + t * (2 * KROW)) = w;
      SB();
    }
  }
  const u32x4 sw = sc.vs, zw = sc.vz;
  WG_BARRIER();
  sc.ks = *(const u32x2*)(c.lds + L_KS + c.ksr + (k + 1) * (DH * 2));
  sc.kz = *(const u32x2*)(c.lds + L_KZ + c.ksr + (k + 1) * (DH * 2));
  sc.vs = *(const u32x4*)(c.lds + L_VS + c.vsr + (k + 1) * (TILE * 2));
  sc.vz = *(const u32x4*)(c.lds + L_VZ + c.vsr + (k + 1) * (TILE * 2));
  SB();
  {
    u32x4 f0, f1, f2, f3;
#pragma unroll
    for (int jj = 0; jj < 4; ++jj) {
      const float sa = bf_lo(sw[jj]), sb = bf_hi(sw[jj]);
      const float ca = -bf_lo(zw[jj]) * sa, cb = -bf_hi(zw[jj]) * sb;
      const u32x4 xa = rv[2 * jj], xb = rv[2 * jj + 1];
      unsigned g0 = pk_bf16(fmaf(i2f(xa[0]), sa, ca), fmaf(i2f(xb[0]), sb, cb));
      unsigned g1 = pk_bf16(fmaf(i2f(xa[1]), sa, ca), fmaf(i2f(xb[1]), sb, cb));
      unsigned g2 = pk_bf16(fmaf(i2f(xa[2]), sa, ca), fmaf(i2f(xb[2]), sb, cb));
      unsigned g3 = pk_bf16(fmaf(i2f(xa[3]), sa, ca), fmaf(i2f(xb[3]), sb, cb));
      asm volatile("" : "+v"(g0), "+v"(g1), "+v"(g2), "+v"(g3));
      f0[jj] = g0; f1[jj] = g1; f2[jj] = g2; f3[jj] = g3;
      SB();
      rk[2 * jj] = ld_k(c, k + 1, 2 * jj);
      rk[2 * jj + 1] = ld_k(c, k + 1, 2 * jj + 1);
      SB();
    }
    char* vb = c.lds + c.vw;
    *(u32x4*)(vb + 0 * 1024) = f0;
    *(u32x4*)(vb + 1 * 1024) = f1;
    *(u32x4*)(vb + 2 * 1024) = f2;
    *(u32x4*)(vb + 3 * 1024) = f3;
  }
  WG_BARRIER();
}

struct ConsState { f32x16 o[4]; float m, l; };

constexpr float RESC_THR = 8.0f;
__device__ __forceinline__ float block_max(const f32x16& s) {
  float a = fmaxf(fmaxf(s[0], s[1]), s[2]), b = fmaxf(fmaxf(s[3], s[4]), s[5]);
  a = fmaxf(fmaxf(a, s[6]), s[7]); b = fmaxf(fmaxf(b, s[8]), s[9]);
  a = fmaxf(fmaxf(a, s[10]), s[11]); b = fmaxf(fmaxf(b, s[12]), s[13]);
  a = fmaxf(fmaxf(a, s[14]), s[15]);
  return xhalf_max(fmaxf(a, b)) * SOFTMAX_C;
}
template <bool PEND>
__device__ __forceinline__ void guard(ConsState& st, float mx, f32x16& pend) {
  if (__builtin_expect(__builtin_amdgcn_ballot_w64(!(mx - st.m <= RESC_THR)) != 0ull, 0)) {
    const float mn = fmaxf(st.m, mx);
    const float alpha = __builtin_amdgcn_exp2f(st.m - mn);
    st.m = mn;
    st.l *= alpha;
#pragma unroll
    for (int i = 0; i < 4; ++i)
#pragma unroll
      for (int g = 0; g < 16; ++g) st.o[i][g] *= alpha;
    if (PEND) {
#pragma unroll
      for (int g = 0; g < 16; ++g) pend[g] *= alpha;
    }
  }
}
__device__ __forceinline__ bf16x8 ldsf(const char* p) { return *(const bf16x8*)p; }
__device__ __forceinline__ f32x16 mfma(bf16x8 a, bf16x8 b, f32x16 c) { return __builtin_amdgcn_mfma_f32_32x32x16_bf16(a, b, c, 0, 0, 0); }
__device__ __forceinline__ bf16x8 pack8(const f32x16& p, int o) {
  u32x4 w = {pk_bf16(p[o], p[o + 1]), pk_bf16(p[o + 2], p[o + 3]), pk_bf16(p[o + 4], p[o + 5]), pk_bf16(p[o + 6], p[o + 7])};
  return __builtin_bit_cast(bf16x8, w);
}
#define EXP2(S, g) do { S[g] = __builtin_amdgcn_exp2f(fmaf(S[g], SOFTMAX_C, mc)); S[(g) + 1] = __builtin_amdgcn_exp2f(fmaf(S[(g) + 1], SOFTMAX_C, mc)); } while (0)
#define SUM2(S, g) do { ls += S[g]; ls += S[(g) + 1]; } while (0)
#define PIN2(S, g) do { float e0_ = S[g], e1_ = S[(g) + 1]; asm volatile("" : "+v"(e0_), "+v"(e1_), "+v"(ls)); S[g] = e0_; S[(g) + 1] = e1_; } while (0)

__device__ __forceinline__ void consume(ConsState& st, const char* lds, const bf16x8 (&qf)[8], unsigned kr, unsigned vr) {
  f32x16 s0, s1;
#pragma unroll
  for (int g = 0; g < 16; ++g) { s0[g] = 0.f; s1[g] = 0.f; }
  bf16x8 fa[8], fb[8];
  WG_BARRIER();
#pragma unroll
  for (int s = 0; s < 8; ++s) fa[s] = ldsf(lds + kr + s * 32);
#pragma unroll
  for (int s = 0; s < 8; ++s) fb[s] = ldsf(lds + kr + 32 * KROW + s * 32);
  SB();
#pragma unroll
  for (int s = 0; s < 8; ++s) s0 = mfma(fa[s], qf[s], s0);
  SB();
  s1 = mfma(fb[0], qf[0], s1);
  SB();
  guard<false>(st, block_max(s0), s0);
  {
    const float mc = -st.m;
    float ls = st.l;
    SB();
#pragma unroll
    for (int s = 1; s < 8; ++s) {
      s1 = mfma(fb[s], qf[s], s1);
      EXP2(s0, 2 * (s - 1));
      if (s > 1) SUM2(s0, 2 * (s - 2));
      PIN2(s0, 2 * (s - 1));
      SB();
    }
    EXP2(s0, 14); SUM2(s0, 12); SUM2(s0, 14);
    st.l = ls;
    SB();
  }
  guard<true>(st, block_max(s1), s0);
  const float mc = -st.m;
  float ls = st.l;
  bf16x8 p0 = pack8(s0, 0), p1 = pack8(s0, 8);
  EXP2(s1, 0); PIN2(s1, 0);
  asm volatile("" : "+v"(p0), "+v"(p1));
  WG_BARRIER();
#pragma unroll
  for (int s = 0; s < 8; ++s) fa[s] = ldsf(lds + vr + s * 1024);
#pragma unroll
  for (int s = 0; s < 8; ++s) fb[s] = ldsf(lds + vr + (8 + s) * 1024);
  EXP2(s1, 2); SUM2(s1, 0); PIN2(s1, 2);
  SB();
  bf16x8 p2;
#pragma unroll
  for (int i = 0; i < 8; ++i) {
    st.o[i & 3] = mfma(fa[i], i < 4 ? p0 : p1, st.o[i & 3]);
    if (i < 6) { EXP2(s1, 2 * i + 4); SUM2(s1, 2 * i + 2); PIN2(s1, 2 * i + 4); }
    if (i == 6) { p2 = pack8(s1, 0); SUM2(s1, 14); asm volatile("" : "+v"(p2), "+v"(ls)); }
    SB();
  }
  st.l = ls;
  st.o[0] = mfma(fb[0], p2, st.o[0]);
  const bf16x8 p3 = pack8(s1, 8);
  SB();
#pragma unroll
  for (int i = 1; i < 8; ++i) st.o[i & 3] = mfma(fb[i], i < 4 ? p2 : p3, st.o[i & 3]);
}

__device__ __forceinline__ float merge4(float a, float wa, float b, float wb, float c, float wc, float d, float wd) {
#pragma clang fp contract(off)
  const float p = a * wa, q = b * wb, r = c * wc, s = d * wd;
  return (p + q) + (r + s);
}
struct Wts { float o, p, a, b; };
__device__ __forceinline__ Wts merge_weights(f32x2 mo, f32x2 mp, f32x2 ma, f32x2 mb) {
  const float M = fmaxf(fmaxf(mo[0], mp[0]), fmaxf(ma[0], mb[0]));
  Wts w;
  w.o = __builtin_amdgcn_exp2f(mo[0] - M); w.p = __builtin_amdgcn_exp2f(mp[0] - M);
  w.a = __builtin_amdgcn_exp2f(ma[0] - M); w.b = __builtin_amdgcn_exp2f(mb[0] - M);
  const float inv = 1.f / merge4(mo[1], w.o, mp[1], w.p, ma[1], w.a, mb[1], w.b);
  w.o *= inv; w.p *= inv; w.a *= inv; w.b *= inv;
  return w;
}
__device__ __forceinline__ u32x4 merge_piece(u32x4 xo, u32x4 xp, u32x4 xa, u32x4 xb, const Wts& w) {
  u32x4 r;
#pragma unroll
  for (int j = 0; j < 4; ++j)
    r[j] = pk_bf16(merge4(bf_lo(xo[j]), w.o, bf_lo(xp[j]), w.p, bf_lo(xa[j]), w.a, bf_lo(xb[j]), w.b),
                   merge4(bf_hi(xo[j]), w.o, bf_hi(xp[j]), w.p, bf_hi(xa[j]), w.a, bf_hi(xb[j]), w.b));
  return r;
}
__device__ __forceinline__ f32x2 ld_ml(rsrc_t rs, int lane, int rec) {
  return __builtin_bit_cast(f32x2, __builtin_amdgcn_raw_buffer_load_b64(rs, lane * 8 + PART_ML, rec, AUX_SC1));
}
__device__ __forceinline__ u32x4 ld_piece(rsrc_t rs, int lane, int e, int rec) {
  return __builtin_amdgcn_raw_buffer_load_b128(rs, lane * 16 + e * 1024, rec, AUX_SC1);
}

__global__ void __launch_bounds__(512, 2)
attn_kernel(const __hip_bfloat16* __restrict__ q, const int* __restrict__ qk, const int* __restrict__ qv,
            const __hip_bfloat16* __restrict__ ksc, const __hip_bfloat16* __restrict__ kzr,
            const __hip_bfloat16* __restrict__ vsc, const __hip_bfloat16* __restrict__ vzr,
            __hip_bfloat16* __restrict__ out, char* ws) {
  extern __shared__ __attribute__((aligned(16))) char lds[];
  const int tid = threadIdx.x, lane = tid & 63;
  const int wave = __builtin_amdgcn_readfirstlane(tid >> 6);
  const int bh = blockIdx.x & (NBH - 1), split = blockIdx.x >> 6;
  const int c = lane & 31, h = lane >> 5;
  const size_t kso = ((size_t)bh * NGK + (size_t)split * TPS) * DH * 2;
  const size_t vso = ((size_t)bh * SKV + (size_t)split * KPS) * 2;

  if (wave < 4) {
    __builtin_amdgcn_s_setprio(3);
    ProdCtx pc;
    const size_t base = ((size_t)bh * SKV + (size_t)split * KPS) * (DH * 4);
    pc.rk = mk_rsrc((const char*)qk + base, KPS * DH * 4);
    pc.rv = mk_rsrc((const char*)qv + base, KPS * DH * 4);
    pc.koff = wave * 8192 + lane * 16;
    pc.voff = wave * 8192 + h * 4096 + c * 16;
    pc.lds = lds;
    pc.kw = L_K + (16 * wave + h) * KROW + c * 8;
    pc.vw = L_V + wave * 4096 + lane * 16;
    pc.ksr = c * 8;
    pc.vsr = (16 * wave + 8 * h) * 2;
    Scales sc;
    sc.ks = ld_nt<u32x2>((const char*)ksc + kso + pc.ksr);
    sc.kz = ld_nt<u32x2>((const char*)kzr + kso + pc.ksr);
    sc.vs = ld_nt<u32x4>((const char*)vsc + vso + pc.vsr);
    sc.vz = ld_nt<u32x4>((const char*)vzr + vso + pc.vsr);
    SB();
    u32x4 rk[8];
#pragma unroll
    for (int i = 0; i < 8; ++i) rk[i] = ld_k(pc, 0, i);
    SB();
    for (int k = 0; k < TPS; ++k) produce(pc, rk, sc, k);
    asm volatile("s_waitcnt vmcnt(0)" ::: "memory");
  } else {
    const int n = wave - 4;
    const int ct = tid - 256;
    gu64* const ew = (gu64*)(ws + (bh * 4 + n) * GRP_BYTES);
    gu64* const cw = ew + 8;
    const u32x4 a0 = ld_nt<u32x4>((const char*)ksc + kso + ct * 16), b0 = ld_nt<u32x4>((const char*)kzr + kso + ct * 16);
    const u32x4 c0 = ld_nt<u32x4>((const char*)vsc + vso + ct * 16), d0 = ld_nt<u32x4>((const char*)vzr + vso + ct * 16);
    const u32x4 a1 = ld_nt<u32x4>((const char*)ksc + kso + 4096 + ct * 16), b1 = ld_nt<u32x4>((const char*)kzr + kso + 4096 + ct * 16);
    SB();
    u32x4 qraw[8];
    {
      const char* qp = (const char*)q + ((size_t)bh * 4 + n) * (SQ * DH * 2) + lane * 16;
#pragma unroll
      for (int i = 0; i < 8; ++i) qraw[i] = ld_nt<u32x4>(qp + i * 1024);
    }
    SB();
    u64 e0v = __hip_atomic_load(ew, RLX_AGENT);
    u64 c0v = __hip_atomic_load(cw, RLX_AGENT);
    SB();
    *(u32x4*)(lds + L_KS + ct * 16) = a0; *(u32x4*)(lds + L_KZ + ct * 16) = b0;
    *(u32x4*)(lds + L_VS + ct * 16) = c0; *(u32x4*)(lds + L_VZ + ct * 16) = d0;
    *(u32x4*)(lds + L_KS + 4096 + ct * 16) = a1; *(u32x4*)(lds + L_KZ + 4096 + ct * 16) = b1;
    bf16x8 qf[8];
    {
      char* qs = lds + L_Q + n * (SQ * KROW);
#pragma unroll
      for (int i = 0; i < 8; ++i) *(u32x4*)(qs + (4 * i + (lane >> 4)) * KROW + (lane & 15) * 16) = qraw[i];
      __builtin_amdgcn_fence(__ATOMIC_RELEASE, "wavefront");
      __builtin_amdgcn_wave_barrier();
      __builtin_amdgcn_fence(__ATOMIC_ACQUIRE, "wavefront");
#pragma unroll
      for (int s = 0; s < 8; ++s) qf[s] = *(const bf16x8*)(qs + c * KROW + h * 16 + s * 32);
    }
    const u64 epoch = uniform64(e0v);
    const u64 tag = ((epoch + 1) * 0x9E3779B97F4A7C15ull) & ~15ull;
    {
      u64 cur = uniform64(c0v);
      const bool mine = (cur & ~15ull) == tag && (cur & 15ull) != 15ull;
      if (!mine && lane == 0) __hip_atomic_compare_exchange_strong(cw, &cur, tag, __ATOMIC_RELAXED, RLX_AGENT);
    }

    const int pr = (c & 0x13) | ((c & 4) << 1) | ((c & 8) >> 1);
    const unsigned kr = L_K + pr * KROW + h * 16;
    const unsigned vr = L_V + lane * 16;
    ConsState st;
#pragma unroll
    for (int i = 0; i < 4; ++i)
#pragma unroll
      for (int g = 0; g < 16; ++g) st.o[i][g] = 0.f;
    st.m = -__builtin_inff(); st.l = 0.f;
    for (int k = 0; k < TPS; ++k) consume(st, lds, qf, kr, vr);

    const f32x2 ml = {st.m, xhalf_sum(st.l)};
    u32x4 own[8];
#pragma unroll
    for (int e = 0; e < 8; ++e)
#pragma unroll
      for (int b = 0; b < 2; ++b) {
        const int g = e * 2 + b;
        own[e][2 * b] = pk_bf16(st.o[0][g], st.o[1][g]);
        own[e][2 * b + 1] = pk_bf16(st.o[2][g], st.o[3][g]);
      }
    const rsrc_t rs = mk_rsrc(ws, WS_BYTES);
    const int cwo = (bh * 4 + n) * GRP_BYTES + 64;
    const bool owner = split == n;
    bool ready = false;
    if (owner) {
#pragma unroll
      for (int e = 0; e < 8; ++e) *(u32x4*)(lds + L_OWN + e * 1024 + lane * 16) = own[e];
      *(f32x2*)(lds + L_OWN + PART_ML + lane * 8) = ml;
      asm volatile("s_waitcnt vmcnt(0)" ::: "memory");
#pragma nounroll
      for (int spin = 0; spin < SPIN_CAP; ++spin) {
        asm volatile("" ::: "memory");
        if ((unsigned)__builtin_amdgcn_readfirstlane(__builtin_amdgcn_raw_buffer_load_b32(rs, cwo, 0, AUX_SC1)) == ((unsigned)tag | 3u)) { ready = true; break; }
        __builtin_amdgcn_s_sleep(1);
      }
      if (lane == 0) *(int*)(lds + L_OWN + PART_BYTES) = ready;
    }
    if (!ready) {
      const int rec0 = (bh * 4 + n) * GRP_BYTES + 128;
      const int rec = rec0 + split * PART_BYTES;
#pragma unroll
      for (int e = 0; e < 8; ++e) __builtin_amdgcn_raw_buffer_store_b128(own[e], rs, lane * 16 + e * 1024, rec, AUX_SC1);
      __builtin_amdgcn_raw_buffer_store_b64(__builtin_bit_cast(u32x2, ml), rs, lane * 8 + PART_ML, rec, AUX_SC1);
      asm volatile("s_waitcnt vmcnt(0)" ::: "memory");
      u64 old = 0;
      if (lane == 0) old = __hip_atomic_fetch_add(cw, owner ? 8ull : 1ull, RLX_AGENT);
      const unsigned o4 = (unsigned)__builtin_amdgcn_readfirstlane((unsigned)old) & 15u;
      if (__builtin_expect(o4 == (owner ? 3u : 10u), 0)) {
        __builtin_amdgcn_fence(__ATOMIC_ACQUIRE, "agent");
        const int rp = rec0 + (split ^ 1) * PART_BYTES, ra = rec0 + ((split & 2) ^ 2) * PART_BYTES, rb = ra + PART_BYTES;
        const Wts w = merge_weights(ml, ld_ml(rs, lane, rp), ld_ml(rs, lane, ra), ld_ml(rs, lane, rb));
        char* op = (char*)out + (size_t)((bh * 4 + n) * SQ + c) * (DH * 2) + h * 32;
#pragma unroll
        for (int e = 0; e < 8; ++e)
          *(u32x4*)(op + (e >> 1) * 64 + (e & 1) * 16) =
              merge_piece(own[e], ld_piece(rs, lane, e, rp), ld_piece(rs, lane, e, ra), ld_piece(rs, lane, e, rb), w);
        if (lane == 0) { __hip_atomic_store(cw, tag | 15ull, RLX_AGENT); __hip_atomic_store(ew, epoch + 1, RLX_AGENT); }
      }
    }
    __syncthreads();
    if (__builtin_amdgcn_readfirstlane(*(const int*)(lds + L_OWN + PART_BYTES))) {
      const int rec0 = (bh * 4 + split) * GRP_BYTES + 128;
      const int rp = rec0 + (split ^ 1) * PART_BYTES, ra = rec0 + ((split & 2) ^ 2) * PART_BYTES, rb = ra + PART_BYTES;
      const f32x2 mp = ld_ml(rs, lane, rp), ma = ld_ml(rs, lane, ra), mb = ld_ml(rs, lane, rb);
      const int e0 = 2 * n;
      const u32x4 xp0 = ld_piece(rs, lane, e0, rp), xa0 = ld_piece(rs, lane, e0, ra), xb0 = ld_piece(rs, lane, e0, rb);
      const u32x4 xp1 = ld_piece(rs, lane, e0 + 1, rp), xa1 = ld_piece(rs, lane, e0 + 1, ra), xb1 = ld_piece(rs, lane, e0 + 1, rb);
      const f32x2 mo = *(const f32x2*)(lds + L_OWN + PART_ML + lane * 8);
      const u32x4 xo0 = *(const u32x4*)(lds + L_OWN + e0 * 1024 + lane * 16);
      const u32x4 xo1 = *(const u32x4*)(lds + L_OWN + e0 * 1024 + 1024 + lane * 16);
      SB();
      const Wts w = merge_weights(mo, mp, ma, mb);
      char* op = (char*)out + (size_t)((bh * 4 + split) * SQ + c) * (DH * 2) + h * 32 + n * 64;
      *(u32x4*)op = merge_piece(xo0, xp0, xa0, xb0, w);
      *(u32x4*)(op + 16) = merge_piece(xo1, xp1, xa1, xb1, w);
      if (owner && lane == 0) { __hip_atomic_store(cw, tag | 15ull, RLX_AGENT); __hip_atomic_store(ew, epoch + 1, RLX_AGENT); }
    }
  }
}

}

extern "C" void kernel_launch(void* const* d_in, const int* in_sizes, int n_in, void* d_out, int out_size,
                              void* d_ws, size_t ws_size, hipStream_t stream) {
  using namespace kvq;
  (void)in_sizes; (void)n_in; (void)out_size; (void)ws_size;
  (void)hipFuncSetAttribute((const void*)attn_kernel, hipFuncAttributeMaxDynamicSharedMemorySize, LDS_BYTES);
  hipLaunchKernelGGL(attn_kernel, dim3(NBH * NSPLIT), dim3(512), LDS_BYTES, stream,
                     (const __hip_bfloat16*)d_in[0], (const int*)d_in[1], (const int*)d_in[2],
                     (const __hip_bfloat16*)d_in[3], (const __hip_bfloat16*)d_in[4],
                     (const __hip_bfloat16*)d_in[5], (const __hip_bfloat16*)d_in[6],
                     (__hip_bfloat16*)d_out, (char*)d_ws);
}
```

```cpp
#define KVQ_WF 8
#define KVQ_PSUB 8
#define KVQ_PACE 2232
#define KVQ_CREDIT 200
#include <hip/hip_runtime.h>
#include <hip/hip_bf16.h>
#include <stdint.h>

namespace kvq {

constexpr int NB = 8, HQ = 32, HKV = 8, SQ = 32, SKV = 8192, DH = 128;
constexpr int TILE = 64;
constexpr int NGK = SKV / TILE;
constexpr int NSPLIT = 4;
constexpr int NBH = NB * HKV;
constexpr int TPS = NGK / NSPLIT;
constexpr int KPS = TPS * TILE;
#define KVQ_PACE0 1000
constexpr int WF = KVQ_WF;
constexpr int SPIN_CAP = 4096;

constexpr int KROW = 272;
constexpr int L_K = 0;
constexpr int L_V = TILE * KROW;
constexpr int L_KS = L_V + 16 * 1024;
constexpr int L_KZ = L_KS + TPS * DH * 2;
constexpr int L_VS = L_KZ + TPS * DH * 2;
constexpr int L_VZ = L_VS + KPS * 2;
constexpr int L_OWN = L_VZ + KPS * 2;
constexpr int L_Q = L_OWN + 8 * 1024 + 512 + 16;
constexpr int LDS_BYTES = L_Q + 4 * SQ * KROW;

constexpr int PART_ML = 8 * 1024;
constexpr int PART_BYTES = PART_ML + 512;
constexpr int GRP_BYTES = 128 + NSPLIT * PART_BYTES;
constexpr int WS_BYTES = NBH * 4 * GRP_BYTES;

constexpr float SOFTMAX_C = 0.08838834764831845f * 1.4426950408889634f;

typedef unsigned u32x4 __attribute__((ext_vector_type(4)));
typedef unsigned u32x2 __attribute__((ext_vector_type(2)));
typedef float f32x2 __attribute__((ext_vector_type(2)));
typedef float f32x16 __attribute__((ext_vector_type(16)));
typedef __bf16 bf16x8 __attribute__((ext_vector_type(8)));
typedef __bf16 bf16x2 __attribute__((ext_vector_type(2)));
typedef unsigned long long u64;
typedef __attribute__((address_space(1))) u64 gu64;
typedef __amdgpu_buffer_rsrc_t rsrc_t;
#define RLX_AGENT __ATOMIC_RELAXED, __HIP_MEMORY_SCOPE_AGENT
#define SB() __builtin_amdgcn_sched_barrier(0)
#define WG_BARRIER() do { SB(); __syncthreads(); SB(); } while (0)
constexpr int AUX_SC1 = 16, AUX_NT = 2;

__device__ __forceinline__ float bf_lo(unsigned w) { return __uint_as_float(w << 16); }
__device__ __forceinline__ float bf_hi(unsigned w) { return __uint_as_float(w & 0xffff0000u); }
__device__ __forceinline__ float i2f(unsigned x) { return (float)(int)x; }
__device__ __forceinline__ unsigned pk_bf16(float a, float b) {
  f32x2 v = {a, b};
  return __builtin_bit_cast(unsigned, __builtin_convertvector(v, bf16x2));
}
__device__ __forceinline__ float xhalf_max(float x) {
  auto r = __builtin_amdgcn_permlane32_swap(__float_as_uint(x), __float_as_uint(x), false, false);
  return fmaxf(__uint_as_float(r[0]), __uint_as_float(r[1]));
}
__device__ __forceinline__ float xhalf_sum(float x) {
  auto r = __builtin_amdgcn_permlane32_swap(__float_as_uint(x), __float_as_uint(x), false, false);
  return __uint_as_float(r[0]) + __uint_as_float(r[1]);
}
template <class T> __device__ __forceinline__ T ld_nt(const void* p) { return __builtin_nontemporal_load((const T*)p); }
__device__ __forceinline__ u64 uniform64(u64 x) {
  const unsigned lo = __builtin_amdgcn_readfirstlane((unsigned)x), hi = __builtin_amdgcn_readfirstlane((unsigned)(x >> 32));
  return ((u64)hi << 32) | lo;
}
__device__ __forceinline__ rsrc_t mk_rsrc(const void* p, int bytes) {
  return __builtin_amdgcn_make_buffer_rsrc((void*)p, 0, bytes, 0x00020000);
}

struct ProdCtx {
  rsrc_t rk, rv;
  unsigned koff, voff;
  char* lds;
  unsigned kw, vw;
  unsigned ksr, vsr;
};
struct Scales { u32x2 ks, kz; u32x4 vs, vz; };

__device__ __forceinline__ u32x4 ld_k(const ProdCtx& c, int t, int i) {
  return __builtin_amdgcn_raw_buffer_load_b128(c.rk, c.koff + t * (TILE * DH * 4) + (i & 3) * 1024, (i >> 2) * 4096, AUX_NT);
}
__device__ __forceinline__ u32x4 ld_v(const ProdCtx& c, int t, int j) {
  return __builtin_amdgcn_raw_buffer_load_b128(c.rv, c.voff + t * (TILE * DH * 4) + j * 512, 0, AUX_NT);
}

__device__ __forceinline__ void pace(unsigned& tn, unsigned step) {
  int lag;
  for (int i = 0;; ++i) { u64 now; asm volatile("s_memrealtime %0\n\ts_waitcnt lgkmcnt(0)" : "=s"(now) :: "memory");
    lag = (int)((unsigned)now * 10u - tn);
    if (lag >= 0 || i >= 64) break; __builtin_amdgcn_s_sleep(1); }
  tn += step + (unsigned)max(lag - KVQ_CREDIT, 0);
}
#define PACE(n) pace(tn, KVQ_PACE / (n))
__device__ __forceinline__ void produce(const ProdCtx& c, u32x4 (&rk)[8], Scales& sc, int k, unsigned& tn) {
  u32x4 rv[8];
  {
    const float s0 = bf_lo(sc.ks[0]), s1 = bf_hi(sc.ks[0]), s2 = bf_lo(sc.ks[1]), s3 = bf_hi(sc.ks[1]);
    const float c0 = -bf_lo(sc.kz[0]) * s0, c1 = -bf_hi(sc.kz[0]) * s1, c2 = -bf_lo(sc.kz[1]) * s2, c3 = -bf_hi(sc.kz[1]) * s3;
    SB();
#pragma unroll
    for (int t = 0; t < 8; ++t) {
      if (t % (16 / KVQ_PSUB) == 0) { PACE(KVQ_PSUB); SB(); }
      const u32x4 x = rk[t];
      u32x2 w;
      w[0] = pk_bf16(fmaf(i2f(x[0]), s0, c0), fmaf(i2f(x[1]), s1, c1));
      w[1] = pk_bf16(fmaf(i2f(x[2]), s2, c2), fmaf(i2f(x[3]), s3, c3));
      asm volatile("" : "+v"(w));
      SB();
      if (t + WF < 8) rk[t + WF] = ld_k(c, k, t + WF); else rv[t + WF - 8] = ld_v(c, k, t + WF - 8);
      *(u32x2*)(c.lds + c.kw + t * (2 * KROW)) = w;
      SB();
    }
  }
  const u32x4 sw = sc.vs, zw = sc.vz;
  WG_BARRIER();
  sc.ks = *(const u32x2*)(c.lds + L_KS + c.ksr + (k + 1) * (DH * 2));
  sc.kz = *(const u32x2*)(c.lds + L_KZ + c.ksr + (k + 1) * (DH * 2));
  sc.vs = *(const u32x4*)(c.lds + L_VS + c.vsr + (k + 1) * (TILE * 2));
  sc.vz = *(const u32x4*)(c.lds + L_VZ + c.vsr + (k + 1) * (TILE * 2));
  SB();
  {
    u32x4 f0, f1, f2, f3;
#pragma unroll
    for (int jj = 0; jj < 4; ++jj) {
      if (KVQ_PSUB == 16) { PACE(8); SB(); } else if (KVQ_PSUB >= 2 && (2 * jj) % (16 / KVQ_PSUB) == 0) { PACE(KVQ_PSUB); SB(); }
      const float sa = bf_lo(sw[jj]), sb = bf_hi(sw[jj]);
      const float ca = -bf_lo(zw[jj]) * sa, cb = -bf_hi(zw[jj]) * sb;
      const u32x4 xa = rv[2 * jj], xb = rv[2 * jj + 1];
      unsigned g0 = pk_bf16(fmaf(i2f(xa[0]), sa, ca), fmaf(i2f(xb[0]), sb, cb));
      unsigned g1 = pk_bf16(fmaf(i2f(xa[1]), sa, ca), fmaf(i2f(xb[1]), sb, cb));
      unsigned g2 = pk_bf16(fmaf(i2f(xa[2]), sa, ca), fmaf(i2f(xb[2]), sb, cb));
      unsigned g3 = pk_bf16(fmaf(i2f(xa[3]), sa, ca), fmaf(i2f(xb[3]), sb, cb));
      asm volatile("" : "+v"(g0), "+v"(g1), "+v"(g2), "+v"(g3));
      f0[jj] = g0; f1[jj] = g1; f2[jj] = g2; f3[jj] = g3;
      SB();
#pragma unroll
      for (int u = 2 * jj + WF; u < 2 * jj + WF + 2; ++u) {
        if (u < 8) rv[u] = ld_v(c, k, u); else rk[u - 8] = ld_k(c, k + 1, u - 8);
      }
      SB();
    }
    char* vb = c.lds + c.vw;
    *(u32x4*)(vb + 0 * 1024) = f0;
    *(u32x4*)(vb + 1 * 1024) = f1;
    *(u32x4*)(vb + 2 * 1024) = f2;
    *(u32x4*)(vb + 3 * 1024) = f3;
  }
  WG_BARRIER();
}

struct ConsState { f32x16 o[4]; float m, l; };

constexpr float RESC_THR = 8.0f;
__device__ __forceinline__ float block_max(const f32x16& s) {
  float a = fmaxf(fmaxf(s[0], s[1]), s[2]), b = fmaxf(fmaxf(s[3], s[4]), s[5]);
  a = fmaxf(fmaxf(a, s[6]), s[7]); b = fmaxf(fmaxf(b, s[8]), s[9]);
  a = fmaxf(fmaxf(a, s[10]), s[11]); b = fmaxf(fmaxf(b, s[12]), s[13]);
  a = fmaxf(fmaxf(a, s[14]), s[15]);
  return xhalf_max(fmaxf(a, b)) * SOFTMAX_C;
}
template <bool PEND>
__device__ __forceinline__ void guard(ConsState& st, float mx, f32x16& pend) {
  if (__builtin_expect(__builtin_amdgcn_ballot_w64(!(mx - st.m <= RESC_THR)) != 0ull, 0)) {
    const float mn = fmaxf(st.m, mx);
    const float alpha = __builtin_amdgcn_exp2f(st.m - mn);
    st.m = mn;
    st.l *= alpha;
#pragma unroll
    for (int i = 0; i < 4; ++i)
#pragma unroll
      for (int g = 0; g < 16; ++g) st.o[i][g] *= alpha;
    if (PEND) {
#pragma unroll
      for (int g = 0; g < 16; ++g) pend[g] *= alpha;
    }
  }
}
__device__ __forceinline__ bf16x8 ldsf(const char* p) { return *(const bf16x8*)p; }
__device__ __forceinline__ f32x16 mfma(bf16x8 a, bf16x8 b, f32x16 c) { return __builtin_amdgcn_mfma_f32_32x32x16_bf16(a, b, c, 0, 0, 0); }
__device__ __forceinline__ bf16x8 pack8(const f32x16& p, int o) {
  u32x4 w = {pk_bf16(p[o], p[o + 1]), pk_bf16(p[o + 2], p[o + 3]), pk_bf16(p[o + 4], p[o + 5]), pk_bf16(p[o + 6], p[o + 7])};
  return __builtin_bit_cast(bf16x8, w);
}
#define EXP2(S, g) do { S[g] = __builtin_amdgcn_exp2f(fmaf(S[g], SOFTMAX_C, mc)); S[(g) + 1] = __builtin_amdgcn_exp2f(fmaf(S[(g) + 1], SOFTMAX_C, mc)); } while (0)
#define SUM2(S, g) do { ls += S[g]; ls += S[(g) + 1]; } while (0)
#define PIN2(S, g) do { float e0_ = S[g], e1_ = S[(g) + 1]; asm volatile("" : "+v"(e0_), "+v"(e1_), "+v"(ls)); S[g] = e0_; S[(g) + 1] = e1_; } while (0)

__device__ __forceinline__ void consume(ConsState& st, const char* lds, const bf16x8 (&qf)[8], unsigned kr, unsigned vr) {
  f32x16 s0, s1;
#pragma unroll
  for (int g = 0; g < 16; ++g) { s0[g] = 0.f; s1[g] = 0.f; }
  bf16x8 fa[8], fb[8];
  WG_BARRIER();
#pragma unroll
  for (int s = 0; s < 8; ++s) fa[s] = ldsf(lds + kr + s * 32);
#pragma unroll
  for (int s = 0; s < 8; ++s) fb[s] = ldsf(lds + kr + 32 * KROW + s * 32);
  SB();
#pragma unroll
  for (int s = 0; s < 8; ++s) s0 = mfma(fa[s], qf[s], s0);
  SB();
  s1 = mfma(fb[0], qf[0], s1);
  SB();
  guard<false>(st, block_max(s0), s0);
  {
    const float mc = -st.m;
    float ls = st.l;
    SB();
#pragma unroll
    for (int s = 1; s < 8; ++s) {
      s1 = mfma(fb[s], qf[s], s1);
      EXP2(s0, 2 * (s - 1));
      if (s > 1) SUM2(s0, 2 * (s - 2));
      PIN2(s0, 2 * (s - 1));
      SB();
    }
    EXP2(s0, 14); SUM2(s0, 12); SUM2(s0, 14);
    st.l = ls;
    SB();
  }
  guard<true>(st, block_max(s1), s0);
  const float mc = -st.m;
  float ls = st.l;
  bf16x8 p0 = pack8(s0, 0), p1 = pack8(s0, 8);
  EXP2(s1, 0); PIN2(s1, 0);
  asm volatile("" : "+v"(p0), "+v"(p1));
  WG_BARRIER();
#pragma unroll
  for (int s = 0; s < 8; ++s) fa[s] = ldsf(lds + vr + s * 1024);
#pragma unroll
  for (int s = 0; s < 8; ++s) fb[s] = ldsf(lds + vr + (8 + s) * 1024);
  EXP2(s1, 2); SUM2(s1, 0); PIN2(s1, 2);
  SB();
  bf16x8 p2;
#pragma unroll
  for (int i = 0; i < 8; ++i) {
    st.o[i & 3] = mfma(fa[i], i < 4 ? p0 : p1, st.o[i & 3]);
    if (i < 6) { EXP2(s1, 2 * i + 4); SUM2(s1, 2 * i + 2); PIN2(s1, 2 * i + 4); }
    if (i == 6) { p2 = pack8(s1, 0); SUM2(s1, 14); asm volatile("" : "+v"(p2), "+v"(ls)); }
    SB();
  }
  st.l = ls;
  st.o[0] = mfma(fb[0], p2, st.o[0]);
  const bf16x8 p3 = pack8(s1, 8);
  SB();
#pragma unroll
  for (int i = 1; i < 8; ++i) st.o[i & 3] = mfma(fb[i], i < 4 ? p2 : p3, st.o[i & 3]);
}

__device__ __forceinline__ float merge4(float a, float wa, float b, float wb, float c, float wc, float d, float wd) {
#pragma clang fp contract(off)
  const float p = a * wa, q = b * wb, r = c * wc, s = d * wd;
  return (p + q) + (r + s);
}
struct Wts { float o, p, a, b; };
__device__ __forceinline__ Wts merge_weights(f32x2 mo, f32x2 mp, f32x2 ma, f32x2 mb) {
  const float M = fmaxf(fmaxf(mo[0], mp[0]), fmaxf(ma[0], mb[0]));
  Wts w;
  w.o = __builtin_amdgcn_exp2f(mo[0] - M); w.p = __builtin_amdgcn_exp2f(mp[0] - M);
  w.a = __builtin_amdgcn_exp2f(ma[0] - M); w.b = __builtin_amdgcn_exp2f(mb[0] - M);
  const float inv = 1.f / merge4(mo[1], w.o, mp[1], w.p, ma[1], w.a, mb[1], w.b);
  w.o *= inv; w.p *= inv; w.a *= inv; w.b *= inv;
  return w;
}
__device__ __forceinline__ u32x4 merge_piece(u32x4 xo, u32x4 xp, u32x4 xa, u32x4 xb, const Wts& w) {
  u32x4 r;
#pragma unroll
  for (int j = 0; j < 4; ++j)
    r[j] = pk_bf16(merge4(bf_lo(xo[j]), w.o, bf_lo(xp[j]), w.p, bf_lo(xa[j]), w.a, bf_lo(xb[j]), w.b),
                   merge4(bf_hi(xo[j]), w.o, bf_hi(xp[j]), w.p, bf_hi(xa[j]), w.a, bf_hi(xb[j]), w.b));
  return r;
}
__device__ __forceinline__ f32x2 ld_ml(rsrc_t rs, int lane, int rec) {
  return __builtin_bit_cast(f32x2, __builtin_amdgcn_raw_buffer_load_b64(rs, lane * 8 + PART_ML, rec, AUX_SC1));
}
__device__ __forceinline__ u32x4 ld_piece(rsrc_t rs, int lane, int e, int rec) {
  return __builtin_amdgcn_raw_buffer_load_b128(rs, lane * 16 + e * 1024, rec, AUX_SC1);
}

__global__ void __launch_bounds__(512, 2)
attn_kernel(const __hip_bfloat16* __restrict__ q, const int* __restrict__ qk, const int* __restrict__ qv,
            const __hip_bfloat16* __restrict__ ksc, const __hip_bfloat16* __restrict__ kzr,
            const __hip_bfloat16* __restrict__ vsc, const __hip_bfloat16* __restrict__ vzr,
            __hip_bfloat16* __restrict__ out, char* ws) {
  extern __shared__ __attribute__((aligned(16))) char lds[];
  const int tid = threadIdx.x, lane = tid & 63;
  const int wave = __builtin_amdgcn_readfirstlane(tid >> 6);
  const int bh = blockIdx.x & (NBH - 1), split = blockIdx.x >> 6;
  const int c = lane & 31, h = lane >> 5;
  const size_t kso = ((size_t)bh * NGK + (size_t)split * TPS) * DH * 2;
  const size_t vso = ((size_t)bh * SKV + (size_t)split * KPS) * 2;

  if (wave < 4) {
    __builtin_amdgcn_s_setprio(3);
    ProdCtx pc;
    const size_t base = ((size_t)bh * SKV + (size_t)split * KPS) * (DH * 4);
    pc.rk = mk_rsrc((const char*)qk + base, KPS * DH * 4);
    pc.rv = mk_rsrc((const char*)qv + base, KPS * DH * 4);
    pc.koff = wave * 8192 + lane * 16;
    pc.voff = wave * 8192 + h * 4096 + c * 16;
    pc.lds = lds;
    pc.kw = L_K + (16 * wave + h) * KROW + c * 8;
    pc.vw = L_V + wave * 4096 + lane * 16;
    pc.ksr = c * 8;
    pc.vsr = (16 * wave + 8 * h) * 2;
    Scales sc;
    sc.ks = ld_nt<u32x2>((const char*)ksc + kso + pc.ksr);
    sc.kz = ld_nt<u32x2>((const char*)kzr + kso + pc.ksr);
    sc.vs = ld_nt<u32x4>((const char*)vsc + vso + pc.vsr);
    sc.vz = ld_nt<u32x4>((const char*)vzr + vso + pc.vsr);
    SB();
    u32x4 rk[8];
#pragma unroll
    for (int i = 0; i < WF; ++i) rk[i] = ld_k(pc, 0, i);
    SB();
    unsigned tn = 0;
    { u64 t0; asm volatile("s_memrealtime %0\n\ts_waitcnt lgkmcnt(0)" : "=s"(t0) :: "memory"); tn = (unsigned)t0 * 10u + KVQ_PACE0; }
    for (int k = 0; k < TPS; ++k) produce(pc, rk, sc, k, tn);
    asm volatile("s_waitcnt vmcnt(0)" ::: "memory");
  } else {
    const int n = wave - 4;
    const int ct = tid - 256;
    gu64* const ew = (gu64*)(ws + (bh * 4 + n) * GRP_BYTES);
    gu64* const cw = ew + 8;
    const u32x4 a0 = ld_nt<u32x4>((const char*)ksc + kso + ct * 16), b0 = ld_nt<u32x4>((const char*)kzr + kso + ct * 16);
    const u32x4 c0 = ld_nt<u32x4>((const char*)vsc + vso + ct * 16), d0 = ld_nt<u32x4>((const char*)vzr + vso + ct * 16);
    const u32x4 a1 = ld_nt<u32x4>((const char*)ksc + kso + 4096 + ct * 16), b1 = ld_nt<u32x4>((const char*)kzr + kso + 4096 + ct * 16);
    SB();
    u32x4 qraw[8];
    {
      const char* qp = (const char*)q + ((size_t)bh * 4 + n) * (SQ * DH * 2) + lane * 16;
#pragma unroll
      for (int i = 0; i < 8; ++i) qraw[i] = ld_nt<u32x4>(qp + i * 1024);
    }
    SB();
    u64 e0v = __hip_atomic_load(ew, RLX_AGENT);
    u64 c0v = __hip_atomic_load(cw, RLX_AGENT);
    SB();
    *(u32x4*)(lds + L_KS + ct * 16) = a0; *(u32x4*)(lds + L_KZ + ct * 16) = b0;
    *(u32x4*)(lds + L_VS + ct * 16) = c0; *(u32x4*)(lds + L_VZ + ct * 16) = d0;
    *(u32x4*)(lds + L_KS + 4096 + ct * 16) = a1; *(u32x4*)(lds + L_KZ + 4096 + ct * 16) = b1;
    bf16x8 qf[8];
    {
      char* qs = lds + L_Q + n * (SQ * KROW);
#pragma unroll
      for (int i = 0; i < 8; ++i) *(u32x4*)(qs + (4 * i + (lane >> 4)) * KROW + (lane & 15) * 16) = qraw[i];
      __builtin_amdgcn_fence(__ATOMIC_RELEASE, "wavefront");
      __builtin_amdgcn_wave_barrier();
      __builtin_amdgcn_fence(__ATOMIC_ACQUIRE, "wavefront");
#pragma unroll
      for (int s = 0; s < 8; ++s) qf[s] = *(const bf16x8*)(qs + c * KROW + h * 16 + s * 32);
    }
    const u64 epoch = uniform64(e0v);
    const u64 tag = ((epoch + 1) * 0x9E3779B97F4A7C15ull) & ~15ull;
    {
      u64 cur = uniform64(c0v);
      const bool mine = (cur & ~15ull) == tag && (cur & 15ull) != 15ull;
      if (!mine && lane == 0) __hip_atomic_compare_exchange_strong(cw, &cur, tag, __ATOMIC_RELAXED, RLX_AGENT);
    }

    const int pr = (c & 0x13) | ((c & 4) << 1) | ((c & 8) >> 1);
    const unsigned kr = L_K + pr * KROW + h * 16;
    const unsigned vr = L_V + lane * 16;
    ConsState st;
#pragma unroll
    for (int i = 0; i < 4; ++i)
#pragma unroll
      for (int g = 0; g < 16; ++g) st.o[i][g] = 0.f;
    st.m = -__builtin_inff(); st.l = 0.f;
    for (int k = 0; k < TPS; ++k) consume(st, lds, qf, kr, vr);

    const f32x2 ml = {st.m, xhalf_sum(st.l)};
    u32x4 own[8];
#pragma unroll
    for (int e = 0; e < 8; ++e)
#pragma unroll
      for (int b = 0; b < 2; ++b) {
        const int g = e * 2 + b;
        own[e][2 * b] = pk_bf16(st.o[0][g], st.o[1][g]);
        own[e][2 * b + 1] = pk_bf16(st.o[2][g], st.o[3][g]);
      }
    const rsrc_t rs = mk_rsrc(ws, WS_BYTES);
    const int cwo = (bh * 4 + n) * GRP_BYTES + 64;
    const bool owner = split == n;
    bool ready = false;
    if (owner) {
#pragma unroll
      for (int e = 0; e < 8; ++e) *(u32x4*)(lds + L_OWN + e * 1024 + lane * 16) = own[e];
      *(f32x2*)(lds + L_OWN + PART_ML + lane * 8) = ml;
      asm volatile("s_waitcnt vmcnt(0)" ::: "memory");
#pragma nounroll
      for (int spin = 0; spin < SPIN_CAP; ++spin) {
        asm volatile("" ::: "memory");
        if ((unsigned)__builtin_amdgcn_readfirstlane(__builtin_amdgcn_raw_buffer_load_b32(rs, cwo, 0, AUX_SC1)) == ((unsigned)tag | 3u)) { ready = true; break; }
        __builtin_amdgcn_s_sleep(1);
      }
      if (lane == 0) *(int*)(lds + L_OWN + PART_BYTES) = ready;
    }
    if (!ready) {
      const int rec0 = (bh * 4 + n) * GRP_BYTES + 128;
      const int rec = rec0 + split * PART_BYTES;
#pragma unroll
      for (int e = 0; e < 8; ++e) __builtin_amdgcn_raw_buffer_store_b128(own[e], rs, lane * 16 + e * 1024, rec, AUX_SC1);
      __builtin_amdgcn_raw_buffer_store_b64(__builtin_bit_cast(u32x2, ml), rs, lane * 8 + PART_ML, rec, AUX_SC1);
      asm volatile("s_waitcnt vmcnt(0)" ::: "memory");
      u64 old = 0;
      if (lane == 0) old = __hip_atomic_fetch_add(cw, owner ? 8ull : 1ull, RLX_AGENT);
      const unsigned o4 = (unsigned)__builtin_amdgcn_readfirstlane((unsigned)old) & 15u;
      if (__builtin_expect(o4 == (owner ? 3u : 10u), 0)) {
        __builtin_amdgcn_fence(__ATOMIC_ACQUIRE, "agent");
        const int rp = rec0 + (split ^ 1) * PART_BYTES, ra = rec0 + ((split & 2) ^ 2) * PART_BYTES, rb = ra + PART_BYTES;
        const Wts w = merge_weights(ml, ld_ml(rs, lane, rp), ld_ml(rs, lane, ra), ld_ml(rs, lane, rb));
        char* op = (char*)out + (size_t)((bh * 4 + n) * SQ + c) * (DH * 2) + h * 32;
#pragma unroll
        for (int e = 0; e < 8; ++e)
          *(u32x4*)(op + (e >> 1) * 64 + (e & 1) * 16) =
              merge_piece(own[e], ld_piece(rs, lane, e, rp), ld_piece(rs, lane, e, ra), ld_piece(rs, lane, e, rb), w);
        if (lane == 0) { __hip_atomic_store(cw, tag | 15ull, RLX_AGENT); __hip_atomic_store(ew, epoch + 1, RLX_AGENT); }
      }
    }
    __syncthreads();
    if (__builtin_amdgcn_readfirstlane(*(const int*)(lds + L_OWN + PART_BYTES))) {
      const int rec0 = (bh * 4 + split) * GRP_BYTES + 128;
      const int rp = rec0 + (split ^ 1) * PART_BYTES, ra = rec0 + ((split & 2) ^ 2) * PART_BYTES, rb = ra + PART_BYTES;
      const f32x2 mp = ld_ml(rs, lane, rp), ma = ld_ml(rs, lane, ra), mb = ld_ml(rs, lane, rb);
      const int e0 = 2 * n;
      const u32x4 xp0 = ld_piece(rs, lane, e0, rp), xa0 = ld_piece(rs, lane, e0, ra), xb0 = ld_piece(rs, lane, e0, rb);
      const u32x4 xp1 = ld_piece(rs, lane, e0 + 1, rp), xa1 = ld_piece(rs, lane, e0 + 1, ra), xb1 = ld_piece(rs, lane, e0 + 1, rb);
      const f32x2 mo = *(const f32x2*)(lds + L_OWN + PART_ML + lane * 8);
      const u32x4 xo0 = *(const u32x4*)(lds + L_OWN + e0 * 1024 + lane * 16);
      const u32x4 xo1 = *(const u32x4*)(lds + L_OWN + e0 * 1024 + 1024 + lane * 16);
      SB();
      const Wts w = merge_weights(mo, mp, ma, mb);
      char* op = (char*)out + (size_t)((bh * 4 + split) * SQ + c) * (DH * 2) + h * 32 + n * 64;
      *(u32x4*)op = merge_piece(xo0, xp0, xa0, xb0, w);
      *(u32x4*)(op + 16) = merge_piece(xo1, xp1, xa1, xb1, w);
      if (owner && lane == 0) { __hip_atomic_store(cw, tag | 15ull, RLX_AGENT); __hip_atomic_store(ew, epoch + 1, RLX_AGENT); }
    }
  }
}

}

extern "C" void kernel_launch(void* const* d_in, const int* in_sizes, int n_in, void* d_out, int out_size,
                              void* d_ws, size_t ws_size, hipStream_t stream) {
  using namespace kvq;
  (void)in_sizes; (void)n_in; (void)out_size; (void)ws_size;
  (void)hipFuncSetAttribute((const void*)attn_kernel, hipFuncAttributeMaxDynamicSharedMemorySize, LDS_BYTES);
  hipLaunchKernelGGL(attn_kernel, dim3(NBH * NSPLIT), dim3(512), LDS_BYTES, stream,
                     (const __hip_bfloat16*)d_in[0], (const int*)d_in[1], (const int*)d_in[2],
                     (const __hip_bfloat16*)d_in[3], (const __hip_bfloat16*)d_in[4],
                     (const __hip_bfloat16*)d_in[5], (const __hip_bfloat16*)d_in[6],
                     (__hip_bfloat16*)d_out, (char*)d_ws);
}
```

```cpp
#define KVQ_WF 8
#define KVQ_LATE 1
#define KVQ_WEFF 5
#define KVQ_WEFF2 6
#include <hip/hip_runtime.h>
#include <hip/hip_bf16.h>
#include <stdint.h>

namespace kvq {

constexpr int NB = 8, HQ = 32, HKV = 8, SQ = 32, SKV = 8192, DH = 128;
constexpr int TILE = 64;
constexpr int NGK = SKV / TILE;
constexpr int NSPLIT = 4;
constexpr int NBH = NB * HKV;
constexpr int TPS = NGK / NSPLIT;
constexpr int KPS = TPS * TILE;
#define KVQ_PACE0 1000
#define KVQ_FLIP 0
constexpr int WF = KVQ_WF;
constexpr int SPIN_CAP = 4096;

constexpr int KROW = 272;
constexpr int L_K = 0;
constexpr int L_V = TILE * KROW;
constexpr int L_KS = L_V + 16 * 1024;
constexpr int L_KZ = L_KS + TPS * DH * 2;
constexpr int L_VS = L_KZ + TPS * DH * 2;
constexpr int L_VZ = L_VS + KPS * 2;
constexpr int L_OWN = L_VZ + KPS * 2;
constexpr int L_Q = L_OWN + 8 * 1024 + 512 + 16;
constexpr int LDS_BYTES = L_Q + 4 * SQ * KROW;

constexpr int PART_ML = 8 * 1024;
constexpr int PART_BYTES = PART_ML + 512;
constexpr int GRP_BYTES = 128 + NSPLIT * PART_BYTES;
constexpr int WS_BYTES = NBH * 4 * GRP_BYTES;

constexpr float SOFTMAX_C = 0.08838834764831845f * 1.4426950408889634f;

typedef unsigned u32x4 __attribute__((ext_vector_type(4)));
typedef unsigned u32x2 __attribute__((ext_vector_type(2)));
typedef float f32x2 __attribute__((ext_vector_type(2)));
typedef float f32x16 __attribute__((ext_vector_type(16)));
typedef __bf16 bf16x8 __attribute__((ext_vector_type(8)));
typedef __bf16 bf16x2 __attribute__((ext_vector_type(2)));
typedef unsigned long long u64;
typedef __attribute__((address_space(1))) u64 gu64;
typedef __amdgpu_buffer_rsrc_t rsrc_t;
#define RLX_AGENT __ATOMIC_RELAXED, __HIP_MEMORY_SCOPE_AGENT
#define SB() __builtin_amdgcn_sched_barrier(0)
#define WG_BARRIER() do { SB(); __syncthreads(); SB(); } while (0)
constexpr int AUX_SC1 = 16, AUX_NT = 2;

__device__ __forceinline__ float bf_lo(unsigned w) { return __uint_as_float(w << 16); }
__device__ __forceinline__ float bf_hi(unsigned w) { return __uint_as_float(w & 0xffff0000u); }
__device__ __forceinline__ float i2f(unsigned x) { return (float)(int)x; }
__device__ __forceinline__ unsigned pk_bf16(float a, float b) {
  f32x2 v = {a, b};
  return __builtin_bit_cast(unsigned, __builtin_convertvector(v, bf16x2));
}
__device__ __forceinline__ float xhalf_max(float x) {
  auto r = __builtin_amdgcn_permlane32_swap(__float_as_uint(x), __float_as_uint(x), false, false);
  return fmaxf(__uint_as_float(r[0]), __uint_as_float(r[1]));
}
__device__ __forceinline__ float xhalf_sum(float x) {
  auto r = __builtin_amdgcn_permlane32_swap(__float_as_uint(x), __float_as_uint(x), false, false);
  return __uint_as_float(r[0]) + __uint_as_float(r[1]);
}
template <class T> __device__ __forceinline__ T ld_nt(const void* p) { return __builtin_nontemporal_load((const T*)p); }
__device__ __forceinline__ u64 uniform64(u64 x) {
  const unsigned lo = __builtin_amdgcn_readfirstlane((unsigned)x), hi = __builtin_amdgcn_readfirstlane((unsigned)(x >> 32));
  return ((u64)hi << 32) | lo;
}
__device__ __forceinline__ rsrc_t mk_rsrc(const void* p, int bytes) {
  return __builtin_amdgcn_make_buffer_rsrc((void*)p, 0, bytes, 0x00020000);
}

struct ProdCtx {
  rsrc_t rk, rv;
  unsigned koff, voff;
  char* lds;
  unsigned kw, vw;
  unsigned ksr, vsr;
  int flip;
};
#define THROTTLE() asm volatile("s_waitcnt vmcnt(%0)" :: "n"(KVQ_WEFF - 1) : "memory")
#define THROTTLE2() asm volatile("s_waitcnt vmcnt(%0)" :: "n"(KVQ_WEFF2 - 1) : "memory")
struct Scales { u32x2 ks, kz; u32x4 vs, vz; };

__device__ __forceinline__ u32x4 ld_k(const ProdCtx& c, int t, int i) {
  return __builtin_amdgcn_raw_buffer_load_b128(c.rk, c.koff + (t ^ c.flip) * (TILE * DH * 4) + (i & 3) * 1024, (i >> 2) * 4096, AUX_NT);
}
__device__ __forceinline__ u32x4 ld_v(const ProdCtx& c, int t, int j) {
  return __builtin_amdgcn_raw_buffer_load_b128(c.rv, c.voff + (t ^ c.flip) * (TILE * DH * 4) + j * 512, 0, AUX_NT);
}

#define PACE(n) do {} while (0)
#define KVQ_PSUB 2
__device__ __forceinline__ void produce(const ProdCtx& c, u32x4 (&rk)[8], Scales& sc, int k, unsigned& tn) {
  u32x4 rv[8];
  {
    const float s0 = bf_lo(sc.ks[0]), s1 = bf_hi(sc.ks[0]), s2 = bf_lo(sc.ks[1]), s3 = bf_hi(sc.ks[1]);
    const float c0 = -bf_lo(sc.kz[0]) * s0, c1 = -bf_hi(sc.kz[0]) * s1, c2 = -bf_lo(sc.kz[1]) * s2, c3 = -bf_hi(sc.kz[1]) * s3;
    SB();
#pragma unroll
    for (int t = 0; t < 8; ++t) {
      if (t % (16 / KVQ_PSUB) == 0) { PACE(KVQ_PSUB); SB(); }
      const u32x4 x = rk[t];
      u32x2 w;
      w[0] = pk_bf16(fmaf(i2f(x[0]), s0, c0), fmaf(i2f(x[1]), s1, c1));
      w[1] = pk_bf16(fmaf(i2f(x[2]), s2, c2), fmaf(i2f(x[3]), s3, c3));
      asm volatile("" : "+v"(w));
      SB();
      *(u32x2*)(c.lds + c.kw + t * (2 * KROW)) = w;
      SB();
      if (t < 7)
      { THROTTLE();
      if (t + WF < 8) rk[t + WF] = ld_k(c, k, t + WF); else rv[t + WF - 8] = ld_v(c, k, t + WF - 8); }
      SB();
    }
  }
  const u32x4 sw = sc.vs, zw = sc.vz;
  WG_BARRIER();
  THROTTLE(); rv[7] = ld_v(c, k, 7); SB();
  sc.ks = *(const u32x2*)(c.lds + L_KS + c.ksr + (((k + 1) ^ c.flip) & (TPS - 1)) * (DH * 2));
  sc.kz = *(const u32x2*)(c.lds + L_KZ + c.ksr + (((k + 1) ^ c.flip) & (TPS - 1)) * (DH * 2));
  sc.vs = *(const u32x4*)(c.lds + L_VS + c.vsr + (((k + 1) ^ c.flip) & (TPS - 1)) * (TILE * 2));
  sc.vz = *(const u32x4*)(c.lds + L_VZ + c.vsr + (((k + 1) ^ c.flip) & (TPS - 1)) * (TILE * 2));
  SB();
  {
    u32x4 f0, f1, f2, f3;
#pragma unroll
    for (int jj = 0; jj < 4; ++jj) {
      if (KVQ_PSUB == 16) { PACE(8); SB(); } else if (KVQ_PSUB >= 2 && (2 * jj) % (16 / KVQ_PSUB) == 0) { PACE(KVQ_PSUB); SB(); }
      const float sa = bf_lo(sw[jj]), sb = bf_hi(sw[jj]);
      const float ca = -bf_lo(zw[jj]) * sa, cb = -bf_hi(zw[jj]) * sb;
      const u32x4 xa = rv[2 * jj], xb = rv[2 * jj + 1];
      unsigned g0 = pk_bf16(fmaf(i2f(xa[0]), sa, ca), fmaf(i2f(xb[0]), sb, cb));
      unsigned g1 = pk_bf16(fmaf(i2f(xa[1]), sa, ca), fmaf(i2f(xb[1]), sb, cb));
      unsigned g2 = pk_bf16(fmaf(i2f(xa[2]), sa, ca), fmaf(i2f(xb[2]), sb, cb));
      unsigned g3 = pk_bf16(fmaf(i2f(xa[3]), sa, ca), fmaf(i2f(xb[3]), sb, cb));
      asm volatile("" : "+v"(g0), "+v"(g1), "+v"(g2), "+v"(g3));
      f0[jj] = g0; f1[jj] = g1; f2[jj] = g2; f3[jj] = g3;
      SB();
#pragma unroll
      for (int u = 2 * jj + WF; u < 2 * jj + WF + 2; ++u) {
        if (jj == 3) continue;
        THROTTLE2();
        if (u < 8) rv[u] = ld_v(c, k, u); else rk[u - 8] = ld_k(c, k + 1, u - 8);
      }
      SB();
    }
    char* vb = c.lds + c.vw;
    *(u32x4*)(vb + 0 * 1024) = f0;
    *(u32x4*)(vb + 1 * 1024) = f1;
    *(u32x4*)(vb + 2 * 1024) = f2;
    *(u32x4*)(vb + 3 * 1024) = f3;
  }
  WG_BARRIER();
  THROTTLE(); rk[6] = ld_k(c, k + 1, 6); THROTTLE(); rk[7] = ld_k(c, k + 1, 7); SB();
}

struct ConsState { f32x16 o[4]; float m, l; };

constexpr float RESC_THR = 8.0f;
__device__ __forceinline__ float block_max(const f32x16& s) {
  float a = fmaxf(fmaxf(s[0], s[1]), s[2]), b = fmaxf(fmaxf(s[3], s[4]), s[5]);
  a = fmaxf(fmaxf(a, s[6]), s[7]); b = fmaxf(fmaxf(b, s[8]), s[9]);
  a = fmaxf(fmaxf(a, s[10]), s[11]); b = fmaxf(fmaxf(b, s[12]), s[13]);
  a = fmaxf(fmaxf(a, s[14]), s[15]);
  return xhalf_max(fmaxf(a, b)) * SOFTMAX_C;
}
template <bool PEND>
__device__ __forceinline__ void guard(ConsState& st, float mx, f32x16& pend) {
  if (__builtin_expect(__builtin_amdgcn_ballot_w64(!(mx - st.m <= RESC_THR)) != 0ull, 0)) {
    const float mn = fmaxf(st.m, mx);
    const float alpha = __builtin_amdgcn_exp2f(st.m - mn);
    st.m = mn;
    st.l *= alpha;
#pragma unroll
    for (int i = 0; i < 4; ++i)
#pragma unroll
      for (int g = 0; g < 16; ++g) st.o[i][g] *= alpha;
    if (PEND) {
#pragma unroll
      for (int g = 0; g < 16; ++g) pend[g] *= alpha;
    }
  }
}
__device__ __forceinline__ bf16x8 ldsf(const char* p) { return *(const bf16x8*)p; }
__device__ __forceinline__ f32x16 mfma(bf16x8 a, bf16x8 b, f32x16 c) { return __builtin_amdgcn_mfma_f32_32x32x16_bf16(a, b, c, 0, 0, 0); }
__device__ __forceinline__ bf16x8 pack8(const f32x16& p, int o) {
  u32x4 w = {pk_bf16(p[o], p[o + 1]), pk_bf16(p[o + 2], p[o + 3]), pk_bf16(p[o + 4], p[o + 5]), pk_bf16(p[o + 6], p[o + 7])};
  return __builtin_bit_cast(bf16x8, w);
}
#define EXP2(S, g) do { S[g] = __builtin_amdgcn_exp2f(fmaf(S[g], SOFTMAX_C, mc)); S[(g) + 1] = __builtin_amdgcn_exp2f(fmaf(S[(g) + 1], SOFTMAX_C, mc)); } while (0)
#define SUM2(S, g) do { ls += S[g]; ls += S[(g) + 1]; } while (0)
#define PIN2(S, g) do { float e0_ = S[g], e1_ = S[(g) + 1]; asm volatile("" : "+v"(e0_), "+v"(e1_), "+v"(ls)); S[g] = e0_; S[(g) + 1] = e1_; } while (0)

__device__ __forceinline__ void consume(ConsState& st, const char* lds, const bf16x8 (&qf)[8], unsigned kr, unsigned vr) {
  f32x16 s0, s1;
#pragma unroll
  for (int g = 0; g < 16; ++g) { s0[g] = 0.f; s1[g] = 0.f; }
  bf16x8 fa[8], fb[8];
  WG_BARRIER();
#pragma unroll
  for (int s = 0; s < 8; ++s) fa[s] = ldsf(lds + kr + s * 32);
#pragma unroll
  for (int s = 0; s < 8; ++s) fb[s] = ldsf(lds + kr + 32 * KROW + s * 32);
  SB();
#pragma unroll
  for (int s = 0; s < 8; ++s) s0 = mfma(fa[s], qf[s], s0);
  SB();
  s1 = mfma(fb[0], qf[0], s1);
  SB();
  guard<false>(st, block_max(s0), s0);
  {
    const float mc = -st.m;
    float ls = st.l;
    SB();
#pragma unroll
    for (int s = 1; s < 8; ++s) {
      s1 = mfma(fb[s], qf[s], s1);
      EXP2(s0, 2 * (s - 1));
      if (s > 1) SUM2(s0, 2 * (s - 2));
      PIN2(s0, 2 * (s - 1));
      SB();
    }
    EXP2(s0, 14); SUM2(s0, 12); SUM2(s0, 14);
    st.l = ls;
    SB();
  }
  guard<true>(st, block_max(s1), s0);
  const float mc = -st.m;
  float ls = st.l;
  bf16x8 p0 = pack8(s0, 0), p1 = pack8(s0, 8);
  EXP2(s1, 0); PIN2(s1, 0);
  asm volatile("" : "+v"(p0), "+v"(p1));
  WG_BARRIER();
#pragma unroll
  for (int s = 0; s < 8; ++s) fa[s] = ldsf(lds + vr + s * 1024);
#pragma unroll
  for (int s = 0; s < 8; ++s) fb[s] = ldsf(lds + vr + (8 + s) * 1024);
  EXP2(s1, 2); SUM2(s1, 0); PIN2(s1, 2);
  SB();
  bf16x8 p2;
#pragma unroll
  for (int i = 0; i < 8; ++i) {
    st.o[i & 3] = mfma(fa[i], i < 4 ? p0 : p1, st.o[i & 3]);
    if (i < 6) { EXP2(s1, 2 * i + 4); SUM2(s1, 2 * i + 2); PIN2(s1, 2 * i + 4); }
    if (i == 6) { p2 = pack8(s1, 0); SUM2(s1, 14); asm volatile("" : "+v"(p2), "+v"(ls)); }
    SB();
  }
  st.l = ls;
  st.o[0] = mfma(fb[0], p2, st.o[0]);
  const bf16x8 p3 = pack8(s1, 8);
  SB();
#pragma unroll
  for (int i = 1; i < 8; ++i) st.o[i & 3] = mfma(fb[i], i < 4 ? p2 : p3, st.o[i & 3]);
}

__device__ __forceinline__ float merge4(float a, float wa, float b, float wb, float c, float wc, float d, float wd) {
#pragma clang fp contract(off)
  const float p = a * wa, q = b * wb, r = c * wc, s = d * wd;
  return (p + q) + (r + s);
}
struct Wts { float o, p, a, b; };
__device__ __forceinline__ Wts merge_weights(f32x2 mo, f32x2 mp, f32x2 ma, f32x2 mb) {
  const float M = fmaxf(fmaxf(mo[0], mp[0]), fmaxf(ma[0], mb[0]));
  Wts w;
  w.o = __builtin_amdgcn_exp2f(mo[0] - M); w.p = __builtin_amdgcn_exp2f(mp[0] - M);
  w.a = __builtin_amdgcn_exp2f(ma[0] - M); w.b = __builtin_amdgcn_exp2f(mb[0] - M);
  const float inv = 1.f / merge4(mo[1], w.o, mp[1], w.p, ma[1], w.a, mb[1], w.b);
  w.o *= inv; w.p *= inv; w.a *= inv; w.b *= inv;
  return w;
}
__device__ __forceinline__ u32x4 merge_piece(u32x4 xo, u32x4 xp, u32x4 xa, u32x4 xb, const Wts& w) {
  u32x4 r;
#pragma unroll
  for (int j = 0; j < 4; ++j)
    r[j] = pk_bf16(merge4(bf_lo(xo[j]), w.o, bf_lo(xp[j]), w.p, bf_lo(xa[j]), w.a, bf_lo(xb[j]), w.b),
                   merge4(bf_hi(xo[j]), w.o, bf_hi(xp[j]), w.p, bf_hi(xa[j]), w.a, bf_hi(xb[j]), w.b));
  return r;
}
__device__ __forceinline__ f32x2 ld_ml(rsrc_t rs, int lane, int rec) {
  return __builtin_bit_cast(f32x2, __builtin_amdgcn_raw_buffer_load_b64(rs, lane * 8 + PART_ML, rec, AUX_SC1));
}
__device__ __forceinline__ u32x4 ld_piece(rsrc_t rs, int lane, int e, int rec) {
  return __builtin_amdgcn_raw_buffer_load_b128(rs, lane * 16 + e * 1024, rec, AUX_SC1);
}

__global__ void __launch_bounds__(512, 2)
attn_kernel(const __hip_bfloat16* __restrict__ q, const int* __restrict__ qk, const int* __restrict__ qv,
            const __hip_bfloat16* __restrict__ ksc, const __hip_bfloat16* __restrict__ kzr,
            const __hip_bfloat16* __restrict__ vsc, const __hip_bfloat16* __restrict__ vzr,
            __hip_bfloat16* __restrict__ out, char* ws) {
  extern __shared__ __attribute__((aligned(16))) char lds[];
  const int tid = threadIdx.x, lane = tid & 63;
  const int wave = __builtin_amdgcn_readfirstlane(tid >> 6);
  const int bh = blockIdx.x & (NBH - 1), split = blockIdx.x >> 6;
  const int c = lane & 31, h = lane >> 5;
  const size_t kso = ((size_t)bh * NGK + (size_t)split * TPS) * DH * 2;
  const size_t vso = ((size_t)bh * SKV + (size_t)split * KPS) * 2;

  if (wave < 4) {
    __builtin_amdgcn_s_setprio(3);
    ProdCtx pc;
    const size_t base = ((size_t)bh * SKV + (size_t)split * KPS) * (DH * 4);
    pc.rk = mk_rsrc((const char*)qk + base, KPS * DH * 4);
    pc.rv = mk_rsrc((const char*)qv + base, KPS * DH * 4);
    pc.koff = wave * 8192 + lane * 16;
    pc.voff = wave * 8192 + h * 4096 + c * 16;
    pc.lds = lds;
    pc.kw = L_K + (16 * wave + h) * KROW + c * 8;
    pc.vw = L_V + wave * 4096 + lane * 16;
    pc.ksr = c * 8;
    pc.vsr = (16 * wave + 8 * h) * 2;
    pc.flip = (KVQ_FLIP && (split & 1)) ? TPS - 1 : 0;
    Scales sc;
    sc.ks = ld_nt<u32x2>((const char*)ksc + kso + pc.ksr + pc.flip * (DH * 2));
    sc.kz = ld_nt<u32x2>((const char*)kzr + kso + pc.ksr + pc.flip * (DH * 2));
    sc.vs = ld_nt<u32x4>((const char*)vsc + vso + pc.vsr + pc.flip * (TILE * 2));
    sc.vz = ld_nt<u32x4>((const char*)vzr + vso + pc.vsr + pc.flip * (TILE * 2));
    SB();
    u32x4 rk[8];
#pragma unroll
    for (int i = 0; i < WF; ++i) rk[i] = ld_k(pc, 0, i);
    SB();
    unsigned tn = 0;
    for (int k = 0; k < TPS; ++k) produce(pc, rk, sc, k, tn);
    asm volatile("s_waitcnt vmcnt(0)" ::: "memory");
  } else {
    const int n = wave - 4;
    const int ct = tid - 256;
    gu64* const ew = (gu64*)(ws + (bh * 4 + n) * GRP_BYTES);
    gu64* const cw = ew + 8;
    const u32x4 a0 = ld_nt<u32x4>((const char*)ksc + kso + ct * 16), b0 = ld_nt<u32x4>((const char*)kzr + kso + ct * 16);
    const u32x4 c0 = ld_nt<u32x4>((const char*)vsc + vso + ct * 16), d0 = ld_nt<u32x4>((const char*)vzr + vso + ct * 16);
    const u32x4 a1 = ld_nt<u32x4>((const char*)ksc + kso + 4096 + ct * 16), b1 = ld_nt<u32x4>((const char*)kzr + kso + 4096 + ct * 16);
    SB();
    u32x4 qraw[8];
    {
      const char* qp = (const char*)q + ((size_t)bh * 4 + n) * (SQ * DH * 2) + lane * 16;
#pragma unroll
      for (int i = 0; i < 8; ++i) qraw[i] = ld_nt<u32x4>(qp + i * 1024);
    }
    SB();
    u64 e0v = __hip_atomic_load(ew, RLX_AGENT);
    u64 c0v = __hip_atomic_load(cw, RLX_AGENT);
    SB();
    *(u32x4*)(lds + L_KS + ct * 16) = a0; *(u32x4*)(lds + L_KZ + ct * 16) = b0;
    *(u32x4*)(lds + L_VS + ct * 16) = c0; *(u32x4*)(lds + L_VZ + ct * 16) = d0;
    *(u32x4*)(lds + L_KS + 4096 + ct * 16) = a1; *(u32x4*)(lds + L_KZ + 4096 + ct * 16) = b1;
    bf16x8 qf[8];
    {
      char* qs = lds + L_Q + n * (SQ * KROW);
#pragma unroll
      for (int i = 0; i < 8; ++i) *(u32x4*)(qs + (4 * i + (lane >> 4)) * KROW + (lane & 15) * 16) = qraw[i];
      __builtin_amdgcn_fence(__ATOMIC_RELEASE, "wavefront");
      __builtin_amdgcn_wave_barrier();
      __builtin_amdgcn_fence(__ATOMIC_ACQUIRE, "wavefront");
#pragma unroll
      for (int s = 0; s < 8; ++s) qf[s] = *(const bf16x8*)(qs + c * KROW + h * 16 + s * 32);
    }
    const u64 epoch = uniform64(e0v);
    const u64 tag = ((epoch + 1) * 0x9E3779B97F4A7C15ull) & ~15ull;
    {
      u64 cur = uniform64(c0v);
      const bool mine = (cur & ~15ull) == tag && (cur & 15ull) != 15ull;
      if (!mine && lane == 0) __hip_atomic_compare_exchange_strong(cw, &cur, tag, __ATOMIC_RELAXED, RLX_AGENT);
    }

    const int pr = (c & 0x13) | ((c & 4) << 1) | ((c & 8) >> 1);
    const unsigned kr = L_K + pr * KROW + h * 16;
    const unsigned vr = L_V + lane * 16;
    ConsState st;
#pragma unroll
    for (int i = 0; i < 4; ++i)
#pragma unroll
      for (int g = 0; g < 16; ++g) st.o[i][g] = 0.f;
    st.m = -__builtin_inff(); st.l = 0.f;
    for (int k = 0; k < TPS; ++k) consume(st, lds, qf, kr, vr);

    const f32x2 ml = {st.m, xhalf_sum(st.l)};
    u32x4 own[8];
#pragma unroll
    for (int e = 0; e < 8; ++e)
#pragma unroll
      for (int b = 0; b < 2; ++b) {
        const int g = e * 2 + b;
        own[e][2 * b] = pk_bf16(st.o[0][g], st.o[1][g]);
        own[e][2 * b + 1] = pk_bf16(st.o[2][g], st.o[3][g]);
      }
    const rsrc_t rs = mk_rsrc(ws, WS_BYTES);
    const int cwo = (bh * 4 + n) * GRP_BYTES + 64;
    const bool owner = split == n;
    bool ready = false;
    if (owner) {
#pragma unroll
      for (int e = 0; e < 8; ++e) *(u32x4*)(lds + L_OWN + e * 1024 + lane * 16) = own[e];
      *(f32x2*)(lds + L_OWN + PART_ML + lane * 8) = ml;
      asm volatile("s_waitcnt vmcnt(0)" ::: "memory");
#pragma nounroll
      for (int spin = 0; spin < SPIN_CAP; ++spin) {
        asm volatile("" ::: "memory");
        if ((unsigned)__builtin_amdgcn_readfirstlane(__builtin_amdgcn_raw_buffer_load_b32(rs, cwo, 0, AUX_SC1)) == ((unsigned)tag | 3u)) { ready = true; break; }
        __builtin_amdgcn_s_sleep(1);
      }
      if (lane == 0) *(int*)(lds + L_OWN + PART_BYTES) = ready;
    }
    if (!ready) {
      const int rec0 = (bh * 4 + n) * GRP_BYTES + 128;
      const int rec = rec0 + split * PART_BYTES;
#pragma unroll
      for (int e = 0; e < 8; ++e) __builtin_amdgcn_raw_buffer_store_b128(own[e], rs, lane * 16 + e * 1024, rec, AUX_SC1);
      __builtin_amdgcn_raw_buffer_store_b64(__builtin_bit_cast(u32x2, ml), rs, lane * 8 + PART_ML, rec, AUX_SC1);
      asm volatile("s_waitcnt vmcnt(0)" ::: "memory");
      u64 old = 0;
      if (lane == 0) old = __hip_atomic_fetch_add(cw, owner ? 8ull : 1ull, RLX_AGENT);
      const unsigned o4 = (unsigned)__builtin_amdgcn_readfirstlane((unsigned)old) & 15u;
      if (__builtin_expect(o4 == (owner ? 3u : 10u), 0)) {
        __builtin_amdgcn_fence(__ATOMIC_ACQUIRE, "agent");
        const int rp = rec0 + (split ^ 1) * PART_BYTES, ra = rec0 + ((split & 2) ^ 2) * PART_BYTES, rb = ra + PART_BYTES;
        const Wts w = merge_weights(ml, ld_ml(rs, lane, rp), ld_ml(rs, lane, ra), ld_ml(rs, lane, rb));
        char* op = (char*)out + (size_t)((bh * 4 + n) * SQ + c) * (DH * 2) + h * 32;
#pragma unroll
        for (int e = 0; e < 8; ++e)
          *(u32x4*)(op + (e >> 1) * 64 + (e & 1) * 16) =
              merge_piece(own[e], ld_piece(rs, lane, e, rp), ld_piece(rs, lane, e, ra), ld_piece(rs, lane, e, rb), w);
        if (lane == 0) { __hip_atomic_store(cw, tag | 15ull, RLX_AGENT); __hip_atomic_store(ew, epoch + 1, RLX_AGENT); }
      }
    }
    __syncthreads();
    if (__builtin_amdgcn_readfirstlane(*(const int*)(lds + L_OWN + PART_BYTES))) {
      const int rec0 = (bh * 4 + split) * GRP_BYTES + 128;
      const int rp = rec0 + (split ^ 1) * PART_BYTES, ra = rec0 + ((split & 2) ^ 2) * PART_BYTES, rb = ra + PART_BYTES;
      const f32x2 mp = ld_ml(rs, lane, rp), ma = ld_ml(rs, lane, ra), mb = ld_ml(rs, lane, rb);
      const int e0 = 2 * n;
      const u32x4 xp0 = ld_piece(rs, lane, e0, rp), xa0 = ld_piece(rs, lane, e0, ra), xb0 = ld_piece(rs, lane, e0, rb);
      const u32x4 xp1 = ld_piece(rs, lane, e0 + 1, rp), xa1 = ld_piece(rs, lane, e0 + 1, ra), xb1 = ld_piece(rs, lane, e0 + 1, rb);
      const f32x2 mo = *(const f32x2*)(lds + L_OWN + PART_ML + lane * 8);
      const u32x4 xo0 = *(const u32x4*)(lds + L_OWN + e0 * 1024 + lane * 16);
      const u32x4 xo1 = *(const u32x4*)(lds + L_OWN + e0 * 1024 + 1024 + lane * 16);
      SB();
      const Wts w = merge_weights(mo, mp, ma, mb);
      char* op = (char*)out + (size_t)((bh * 4 + split) * SQ + c) * (DH * 2) + h * 32 + n * 64;
      *(u32x4*)op = merge_piece(xo0, xp0, xa0, xb0, w);
      *(u32x4*)(op + 16) = merge_piece(xo1, xp1, xa1, xb1, w);
      if (owner && lane == 0) { __hip_atomic_store(cw, tag | 15ull, RLX_AGENT); __hip_atomic_store(ew, epoch + 1, RLX_AGENT); }
    }
  }
}

}

extern "C" void kernel_launch(void* const* d_in, const int* in_sizes, int n_in, void* d_out, int out_size,
                              void* d_ws, size_t ws_size, hipStream_t stream) {
  using namespace kvq;
  (void)in_sizes; (void)n_in; (void)out_size; (void)ws_size;
  (void)hipFuncSetAttribute((const void*)attn_kernel, hipFuncAttributeMaxDynamicSharedMemorySize, LDS_BYTES);
  hipLaunchKernelGGL(attn_kernel, dim3(NBH * NSPLIT), dim3(512), LDS_BYTES, stream,
                     (const __hip_bfloat16*)d_in[0], (const int*)d_in[1], (const int*)d_in[2],
                     (const __hip_bfloat16*)d_in[3], (const __hip_bfloat16*)d_in[4],
                     (const __hip_bfloat16*)d_in[5], (const __hip_bfloat16*)d_in[6],
                     (__hip_bfloat16*)d_out, (char*)d_ws);
}
```

```cpp
#define KVQ_WF 8
#define KVQ_LATE 1
#define KVQ_WEFF 6
#define KVQ_PSUB 8
#define KVQ_PACE 2232
#define KVQ_CREDIT 200
#include <hip/hip_runtime.h>
#include <hip/hip_bf16.h>
#include <stdint.h>

namespace kvq {

constexpr int NB = 8, HQ = 32, HKV = 8, SQ = 32, SKV = 8192, DH = 128;
constexpr int TILE = 64;
constexpr int NGK = SKV / TILE;
constexpr int NSPLIT = 4;
constexpr int NBH = NB * HKV;
constexpr int TPS = NGK / NSPLIT;
constexpr int KPS = TPS * TILE;
#define KVQ_PACE0 1000
#define KVQ_FLIP 0
constexpr int WF = KVQ_WF;
constexpr int SPIN_CAP = 4096;

constexpr int KROW = 272;
constexpr int L_K = 0;
constexpr int L_V = TILE * KROW;
constexpr int L_KS = L_V + 16 * 1024;
constexpr int L_KZ = L_KS + TPS * DH * 2;
constexpr int L_VS = L_KZ + TPS * DH * 2;
constexpr int L_VZ = L_VS + KPS * 2;
constexpr int L_OWN = L_VZ + KPS * 2;
constexpr int L_Q = L_OWN + 8 * 1024 + 512 + 16;
constexpr int LDS_BYTES = L_Q + 4 * SQ * KROW;

constexpr int PART_ML = 8 * 1024;
constexpr int PART_BYTES = PART_ML + 512;
constexpr int GRP_BYTES = 128 + NSPLIT * PART_BYTES;
constexpr int WS_BYTES = NBH * 4 * GRP_BYTES;

constexpr float SOFTMAX_C = 0.08838834764831845f * 1.4426950408889634f;

typedef unsigned u32x4 __attribute__((ext_vector_type(4)));
typedef unsigned u32x2 __attribute__((ext_vector_type(2)));
typedef float f32x2 __attribute__((ext_vector_type(2)));
typedef float f32x16 __attribute__((ext_vector_type(16)));
typedef __bf16 bf16x8 __attribute__((ext_vector_type(8)));
typedef __bf16 bf16x2 __attribute__((ext_vector_type(2)));
typedef unsigned long long u64;
typedef __attribute__((address_space(1))) u64 gu64;
typedef __amdgpu_buffer_rsrc_t rsrc_t;
#define RLX_AGENT __ATOMIC_RELAXED, __HIP_MEMORY_SCOPE_AGENT
#define SB() __builtin_amdgcn_sched_barrier(0)
#define WG_BARRIER() do { SB(); __syncthreads(); SB(); } while (0)
constexpr int AUX_SC1 = 16, AUX_NT = 2;

__device__ __forceinline__ float bf_lo(unsigned w) { return __uint_as_float(w << 16); }
__device__ __forceinline__ float bf_hi(unsigned w) { return __uint_as_float(w & 0xffff0000u); }
__device__ __forceinline__ float i2f(unsigned x) { return (float)(int)x; }
__device__ __forceinline__ unsigned pk_bf16(float a, float b) {
  f32x2 v = {a, b};
  return __builtin_bit_cast(unsigned, __builtin_convertvector(v, bf16x2));
}
__device__ __forceinline__ float xhalf_max(float x) {
  auto r = __builtin_amdgcn_permlane32_swap(__float_as_uint(x), __float_as_uint(x), false, false);
  return fmaxf(__uint_as_float(r[0]), __uint_as_float(r[1]));
}
__device__ __forceinline__ float xhalf_sum(float x) {
  auto r = __builtin_amdgcn_permlane32_swap(__float_as_uint(x), __float_as_uint(x), false, false);
  return __uint_as_float(r[0]) + __uint_as_float(r[1]);
}
template <class T> __device__ __forceinline__ T ld_nt(const void* p) { return __builtin_nontemporal_load((const T*)p); }
__device__ __forceinline__ u64 uniform64(u64 x) {
  const unsigned lo = __builtin_amdgcn_readfirstlane((unsigned)x), hi = __builtin_amdgcn_readfirstlane((unsigned)(x >> 32));
  return ((u64)hi << 32) | lo;
}
__device__ __forceinline__ rsrc_t mk_rsrc(const void* p, int bytes) {
  return __builtin_amdgcn_make_buffer_rsrc((void*)p, 0, bytes, 0x00020000);
}

struct ProdCtx {
  rsrc_t rk, rv;
  unsigned koff, voff;
  char* lds;
  unsigned kw, vw;
  unsigned ksr, vsr;
  int flip;
};
#define KVQ_WEFF2 KVQ_WEFF
#define THROTTLE() asm volatile("s_waitcnt vmcnt(%0)" :: "n"(KVQ_WEFF - 1) : "memory")
#define THROTTLE2() asm volatile("s_waitcnt vmcnt(%0)" :: "n"(KVQ_WEFF2 - 1) : "memory")
struct Scales { u32x2 ks, kz; u32x4 vs, vz; };

__device__ __forceinline__ u32x4 ld_k(const ProdCtx& c, int t, int i) {
  return __builtin_amdgcn_raw_buffer_load_b128(c.rk, c.koff + (t ^ c.flip) * (TILE * DH * 4) + (i & 3) * 1024, (i >> 2) * 4096, AUX_NT);
}
__device__ __forceinline__ u32x4 ld_v(const ProdCtx& c, int t, int j) {
  return __builtin_amdgcn_raw_buffer_load_b128(c.rv, c.voff + (t ^ c.flip) * (TILE * DH * 4) + j * 512, 0, AUX_NT);
}

__device__ __forceinline__ void pace(unsigned& tn, unsigned step) {
  int lag;
  for (int i = 0;; ++i) { u64 now; asm volatile("s_memrealtime %0\n\ts_waitcnt lgkmcnt(0)" : "=s"(now) :: "memory");
    lag = (int)((unsigned)now * 10u - tn);
    if (lag >= 0 || i >= 64) break; __builtin_amdgcn_s_sleep(1); }
  tn += step + (unsigned)max(lag - KVQ_CREDIT, 0);
}
#define PACE(n) pace(tn, KVQ_PACE / (n))
__device__ __forceinline__ void produce(const ProdCtx& c, u32x4 (&rk)[8], Scales& sc, int k, unsigned& tn) {
  u32x4 rv[8];
  {
    const float s0 = bf_lo(sc.ks[0]), s1 = bf_hi(sc.ks[0]), s2 = bf_lo(sc.ks[1]), s3 = bf_hi(sc.ks[1]);
    const float c0 = -bf_lo(sc.kz[0]) * s0, c1 = -bf_hi(sc.kz[0]) * s1, c2 = -bf_lo(sc.kz[1]) * s2, c3 = -bf_hi(sc.kz[1]) * s3;
    SB();
#pragma unroll
    for (int t = 0; t < 8; ++t) {
      if (t % (16 / KVQ_PSUB) == 0) { PACE(KVQ_PSUB); SB(); }
      const u32x4 x = rk[t];
      u32x2 w;
      w[0] = pk_bf16(fmaf(i2f(x[0]), s0, c0), fmaf(i2f(x[1]), s1, c1));
      w[1] = pk_bf16(fmaf(i2f(x[2]), s2, c2), fmaf(i2f(x[3]), s3, c3));
      asm volatile("" : "+v"(w));
      SB();
      *(u32x2*)(c.lds + c.kw + t * (2 * KROW)) = w;
      SB();
      if (t < 7)
      { THROTTLE();
      if (t + WF < 8) rk[t + WF] = ld_k(c, k, t + WF); else rv[t + WF - 8] = ld_v(c, k, t + WF - 8); }
      SB();
    }
  }
  const u32x4 sw = sc.vs, zw = sc.vz;
  WG_BARRIER();
  THROTTLE(); rv[7] = ld_v(c, k, 7); SB();
  sc.ks = *(const u32x2*)(c.lds + L_KS + c.ksr + (((k + 1) ^ c.flip) & (TPS - 1)) * (DH * 2));
  sc.kz = *(const u32x2*)(c.lds + L_KZ + c.ksr + (((k + 1) ^ c.flip) & (TPS - 1)) * (DH * 2));
  sc.vs = *(const u32x4*)(c.lds + L_VS + c.vsr + (((k + 1) ^ c.flip) & (TPS - 1)) * (TILE * 2));
  sc.vz = *(const u32x4*)(c.lds + L_VZ + c.vsr + (((k + 1) ^ c.flip) & (TPS - 1)) * (TILE * 2));
  SB();
  {
    u32x4 f0, f1, f2, f3;
#pragma unroll
    for (int jj = 0; jj < 4; ++jj) {
      if (KVQ_PSUB == 16) { PACE(8); SB(); } else if (KVQ_PSUB >= 2 && (2 * jj) % (16 / KVQ_PSUB) == 0) { PACE(KVQ_PSUB); SB(); }
      const float sa = bf_lo(sw[jj]), sb = bf_hi(sw[jj]);
      const float ca = -bf_lo(zw[jj]) * sa, cb = -bf_hi(zw[jj]) * sb;
      const u32x4 xa = rv[2 * jj], xb = rv[2 * jj + 1];
      unsigned g0 = pk_bf16(fmaf(i2f(xa[0]), sa, ca), fmaf(i2f(xb[0]), sb, cb));
      unsigned g1 = pk_bf16(fmaf(i2f(xa[1]), sa, ca), fmaf(i2f(xb[1]), sb, cb));
      unsigned g2 = pk_bf16(fmaf(i2f(xa[2]), sa, ca), fmaf(i2f(xb[2]), sb, cb));
      unsigned g3 = pk_bf16(fmaf(i2f(xa[3]), sa, ca), fmaf(i2f(xb[3]), sb, cb));
      asm volatile("" : "+v"(g0), "+v"(g1), "+v"(g2), "+v"(g3));
      f0[jj] = g0; f1[jj] = g1; f2[jj] = g2; f3[jj] = g3;
      SB();
#pragma unroll
      for (int u = 2 * jj + WF; u < 2 * jj + WF + 2; ++u) {
        if (jj == 3) continue;
        THROTTLE2();
        if (u < 8) rv[u] = ld_v(c, k, u); else rk[u - 8] = ld_k(c, k + 1, u - 8);
      }
      SB();
    }
    char* vb = c.lds + c.vw;
    *(u32x4*)(vb + 0 * 1024) = f0;
    *(u32x4*)(vb + 1 * 1024) = f1;
    *(u32x4*)(vb + 2 * 1024) = f2;
    *(u32x4*)(vb + 3 * 1024) = f3;
  }
  WG_BARRIER();
  THROTTLE(); rk[6] = ld_k(c, k + 1, 6); THROTTLE(); rk[7] = ld_k(c, k + 1, 7); SB();
}

struct ConsState { f32x16 o[4]; float m, l; };

constexpr float RESC_THR = 8.0f;
__device__ __forceinline__ float block_max(const f32x16& s) {
  float a = fmaxf(fmaxf(s[0], s[1]), s[2]), b = fmaxf(fmaxf(s[3], s[4]), s[5]);
  a = fmaxf(fmaxf(a, s[6]), s[7]); b = fmaxf(fmaxf(b, s[8]), s[9]);
  a = fmaxf(fmaxf(a, s[10]), s[11]); b = fmaxf(fmaxf(b, s[12]), s[13]);
  a = fmaxf(fmaxf(a, s[14]), s[15]);
  return xhalf_max(fmaxf(a, b)) * SOFTMAX_C;
}
template <bool PEND>
__device__ __forceinline__ void guard(ConsState& st, float mx, f32x16& pend) {
  if (__builtin_expect(__builtin_amdgcn_ballot_w64(!(mx - st.m <= RESC_THR)) != 0ull, 0)) {
    const float mn = fmaxf(st.m, mx);
    const float alpha = __builtin_amdgcn_exp2f(st.m - mn);
    st.m = mn;
    st.l *= alpha;
#pragma unroll
    for (int i = 0; i < 4; ++i)
#pragma unroll
      for (int g = 0; g < 16; ++g) st.o[i][g] *= alpha;
    if (PEND) {
#pragma unroll
      for (int g = 0; g < 16; ++g) pend[g] *= alpha;
    }
  }
}
__device__ __forceinline__ bf16x8 ldsf(const char* p) { return *(const bf16x8*)p; }
__device__ __forceinline__ f32x16 mfma(bf16x8 a, bf16x8 b, f32x16 c) { return __builtin_amdgcn_mfma_f32_32x32x16_bf16(a, b, c, 0, 0, 0); }
__device__ __forceinline__ bf16x8 pack8(const f32x16& p, int o) {
  u32x4 w = {pk_bf16(p[o], p[o + 1]), pk_bf16(p[o + 2], p[o + 3]), pk_bf16(p[o + 4], p[o + 5]), pk_bf16(p[o + 6], p[o + 7])};
  return __builtin_bit_cast(bf16x8, w);
}
#define EXP2(S, g) do { S[g] = __builtin_amdgcn_exp2f(fmaf(S[g], SOFTMAX_C, mc)); S[(g) + 1] = __builtin_amdgcn_exp2f(fmaf(S[(g) + 1], SOFTMAX_C, mc)); } while (0)
#define SUM2(S, g) do { ls += S[g]; ls += S[(g) + 1]; } while (0)
#define PIN2(S, g) do { float e0_ = S[g], e1_ = S[(g) + 1]; asm volatile("" : "+v"(e0_), "+v"(e1_), "+v"(ls)); S[g] = e0_; S[(g) + 1] = e1_; } while (0)

__device__ __forceinline__ void consume(ConsState& st, const char* lds, const bf16x8 (&qf)[8], unsigned kr, unsigned vr) {
  f32x16 s0, s1;
#pragma unroll
  for (int g = 0; g < 16; ++g) { s0[g] = 0.f; s1[g] = 0.f; }
  bf16x8 fa[8], fb[8];
  WG_BARRIER();
#pragma unroll
  for (int s = 0; s < 8; ++s) fa[s] = ldsf(lds + kr + s * 32);
#pragma unroll
  for (int s = 0; s < 8; ++s) fb[s] = ldsf(lds + kr + 32 * KROW + s * 32);
  SB();
#pragma unroll
  for (int s = 0; s < 8; ++s) s0 = mfma(fa[s], qf[s], s0);
  SB();
  s1 = mfma(fb[0], qf[0], s1);
  SB();
  guard<false>(st, block_max(s0), s0);
  {
    const float mc = -st.m;
    float ls = st.l;
    SB();
#pragma unroll
    for (int s = 1; s < 8; ++s) {
      s1 = mfma(fb[s], qf[s], s1);
      EXP2(s0, 2 * (s - 1));
      if (s > 1) SUM2(s0, 2 * (s - 2));
      PIN2(s0, 2 * (s - 1));
      SB();
    }
    EXP2(s0, 14); SUM2(s0, 12); SUM2(s0, 14);
    st.l = ls;
    SB();
  }
  guard<true>(st, block_max(s1), s0);
  const float mc = -st.m;
  float ls = st.l;
  bf16x8 p0 = pack8(s0, 0), p1 = pack8(s0, 8);
  EXP2(s1, 0); PIN2(s1, 0);
  asm volatile("" : "+v"(p0), "+v"(p1));
  WG_BARRIER();
#pragma unroll
  for (int s = 0; s < 8; ++s) fa[s] = ldsf(lds + vr + s * 1024);
#pragma unroll
  for (int s = 0; s < 8; ++s) fb[s] = ldsf(lds + vr + (8 + s) * 1024);
  EXP2(s1, 2); SUM2(s1, 0); PIN2(s1, 2);
  SB();
  bf16x8 p2;
#pragma unroll
  for (int i = 0; i < 8; ++i) {
    st.o[i & 3] = mfma(fa[i], i < 4 ? p0 : p1, st.o[i & 3]);
    if (i < 6) { EXP2(s1, 2 * i + 4); SUM2(s1, 2 * i + 2); PIN2(s1, 2 * i + 4); }
    if (i == 6) { p2 = pack8(s1, 0); SUM2(s1, 14); asm volatile("" : "+v"(p2), "+v"(ls)); }
    SB();
  }
  st.l = ls;
  st.o[0] = mfma(fb[0], p2, st.o[0]);
  const bf16x8 p3 = pack8(s1, 8);
  SB();
#pragma unroll
  for (int i = 1; i < 8; ++i) st.o[i & 3] = mfma(fb[i], i < 4 ? p2 : p3, st.o[i & 3]);
}

__device__ __forceinline__ float merge4(float a, float wa, float b, float wb, float c, float wc, float d, float wd) {
#pragma clang fp contract(off)
  const float p = a * wa, q = b * wb, r = c * wc, s = d * wd;
  return (p + q) + (r + s);
}
struct Wts { float o, p, a, b; };
__device__ __forceinline__ Wts merge_weights(f32x2 mo, f32x2 mp, f32x2 ma, f32x2 mb) {
  const float M = fmaxf(fmaxf(mo[0], mp[0]), fmaxf(ma[0], mb[0]));
  Wts w;
  w.o = __builtin_amdgcn_exp2f(mo[0] - M); w.p = __builtin_amdgcn_exp2f(mp[0] - M);
  w.a = __builtin_amdgcn_exp2f(ma[0] - M); w.b = __builtin_amdgcn_exp2f(mb[0] - M);
  const float inv = 1.f / merge4(mo[1], w.o, mp[1], w.p, ma[1], w.a, mb[1], w.b);
  w.o *= inv; w.p *= inv; w.a *= inv; w.b *= inv;
  return w;
}
__device__ __forceinline__ u32x4 merge_piece(u32x4 xo, u32x4 xp, u32x4 xa, u32x4 xb, const Wts& w) {
  u32x4 r;
#pragma unroll
  for (int j = 0; j < 4; ++j)
    r[j] = pk_bf16(merge4(bf_lo(xo[j]), w.o, bf_lo(xp[j]), w.p, bf_lo(xa[j]), w.a, bf_lo(xb[j]), w.b),
                   merge4(bf_hi(xo[j]), w.o, bf_hi(xp[j]), w.p, bf_hi(xa[j]), w.a, bf_hi(xb[j]), w.b));
  return r;
}
__device__ __forceinline__ f32x2 ld_ml(rsrc_t rs, int lane, int rec) {
  return __builtin_bit_cast(f32x2, __builtin_amdgcn_raw_buffer_load_b64(rs, lane * 8 + PART_ML, rec, AUX_SC1));
}
__device__ __forceinline__ u32x4 ld_piece(rsrc_t rs, int lane, int e, int rec) {
  return __builtin_amdgcn_raw_buffer_load_b128(rs, lane * 16 + e * 1024, rec, AUX_SC1);
}

__global__ void __launch_bounds__(512, 2)
attn_kernel(const __hip_bfloat16* __restrict__ q, const int* __restrict__ qk, const int* __restrict__ qv,
            const __hip_bfloat16* __restrict__ ksc, const __hip_bfloat16* __restrict__ kzr,
            const __hip_bfloat16* __restrict__ vsc, const __hip_bfloat16* __restrict__ vzr,
            __hip_bfloat16* __restrict__ out, char* ws) {
  extern __shared__ __attribute__((aligned(16))) char lds[];
  const int tid = threadIdx.x, lane = tid & 63;
  const int wave = __builtin_amdgcn_readfirstlane(tid >> 6);
  const int bh = blockIdx.x & (NBH - 1), split = blockIdx.x >> 6;
  const int c = lane & 31, h = lane >> 5;
  const size_t kso = ((size_t)bh * NGK + (size_t)split * TPS) * DH * 2;
  const size_t vso = ((size_t)bh * SKV + (size_t)split * KPS) * 2;

  if (wave < 4) {
    __builtin_amdgcn_s_setprio(3);
    ProdCtx pc;
    const size_t base = ((size_t)bh * SKV + (size_t)split * KPS) * (DH * 4);
    pc.rk = mk_rsrc((const char*)qk + base, KPS * DH * 4);
    pc.rv = mk_rsrc((const char*)qv + base, KPS * DH * 4);
    pc.koff = wave * 8192 + lane * 16;
    pc.voff = wave * 8192 + h * 4096 + c * 16;
    pc.lds = lds;
    pc.kw = L_K + (16 * wave + h) * KROW + c * 8;
    pc.vw = L_V + wave * 4096 + lane * 16;
    pc.ksr = c * 8;
    pc.vsr = (16 * wave + 8 * h) * 2;
    pc.flip = (KVQ_FLIP && (split & 1)) ? TPS - 1 : 0;
    Scales sc;
    sc.ks = ld_nt<u32x2>((const char*)ksc + kso + pc.ksr + pc.flip * (DH * 2));
    sc.kz = ld_nt<u32x2>((const char*)kzr + kso + pc.ksr + pc.flip * (DH * 2));
    sc.vs = ld_nt<u32x4>((const char*)vsc + vso + pc.vsr + pc.flip * (TILE * 2));
    sc.vz = ld_nt<u32x4>((const char*)vzr + vso + pc.vsr + pc.flip * (TILE * 2));
    SB();
    u32x4 rk[8];
#pragma unroll
    for (int i = 0; i < WF; ++i) rk[i] = ld_k(pc, 0, i);
    SB();
    unsigned tn = 0;
    { u64 t0; asm volatile("s_memrealtime %0\n\ts_waitcnt lgkmcnt(0)" : "=s"(t0) :: "memory"); tn = (unsigned)t0 * 10u + KVQ_PACE0; }
    for (int k = 0; k < TPS; ++k) produce(pc, rk, sc, k, tn);
    asm volatile("s_waitcnt vmcnt(0)" ::: "memory");
  } else {
    const int n = wave - 4;
    const int ct = tid - 256;
    gu64* const ew = (gu64*)(ws + (bh * 4 + n) * GRP_BYTES);
    gu64* const cw = ew + 8;
    const u32x4 a0 = ld_nt<u32x4>((const char*)ksc + kso + ct * 16), b0 = ld_nt<u32x4>((const char*)kzr + kso + ct * 16);
    const u32x4 c0 = ld_nt<u32x4>((const char*)vsc + vso + ct * 16), d0 = ld_nt<u32x4>((const char*)vzr + vso + ct * 16);
    const u32x4 a1 = ld_nt<u32x4>((const char*)ksc + kso + 4096 + ct * 16), b1 = ld_nt<u32x4>((const char*)kzr + kso + 4096 + ct * 16);
    SB();
    u32x4 qraw[8];
    {
      const char* qp = (const char*)q + ((size_t)bh * 4 + n) * (SQ * DH * 2) + lane * 16;
#pragma unroll
      for (int i = 0; i < 8; ++i) qraw[i] = ld_nt<u32x4>(qp + i * 1024);
    }
    SB();
    u64 e0v = __hip_atomic_load(ew, RLX_AGENT);
    u64 c0v = __hip_atomic_load(cw, RLX_AGENT);
    SB();
    *(u32x4*)(lds + L_KS + ct * 16) = a0; *(u32x4*)(lds + L_KZ + ct * 16) = b0;
    *(u32x4*)(lds + L_VS + ct * 16) = c0; *(u32x4*)(lds + L_VZ + ct * 16) = d0;
    *(u32x4*)(lds + L_KS + 4096 + ct * 16) = a1; *(u32x4*)(lds + L_KZ + 4096 + ct * 16) = b1;
    bf16x8 qf[8];
    {
      char* qs = lds + L_Q + n * (SQ * KROW);
#pragma unroll
      for (int i = 0; i < 8; ++i) *(u32x4*)(qs + (4 * i + (lane >> 4)) * KROW + (lane & 15) * 16) = qraw[i];
      __builtin_amdgcn_fence(__ATOMIC_RELEASE, "wavefront");
      __builtin_amdgcn_wave_barrier();
      __builtin_amdgcn_fence(__ATOMIC_ACQUIRE, "wavefront");
#pragma unroll
      for (int s = 0; s < 8; ++s) qf[s] = *(const bf16x8*)(qs + c * KROW + h * 16 + s * 32);
    }
    const u64 epoch = uniform64(e0v);
    const u64 tag = ((epoch + 1) * 0x9E3779B97F4A7C15ull) & ~15ull;
    {
      u64 cur = uniform64(c0v);
      const bool mine = (cur & ~15ull) == tag && (cur & 15ull) != 15ull;
      if (!mine && lane == 0) __hip_atomic_compare_exchange_strong(cw, &cur, tag, __ATOMIC_RELAXED, RLX_AGENT);
    }

    const int pr = (c & 0x13) | ((c & 4) << 1) | ((c & 8) >> 1);
    const unsigned kr = L_K + pr * KROW + h * 16;
    const unsigned vr = L_V + lane * 16;
    ConsState st;
#pragma unroll
    for (int i = 0; i < 4; ++i)
#pragma unroll
      for (int g = 0; g < 16; ++g) st.o[i][g] = 0.f;
    st.m = -__builtin_inff(); st.l = 0.f;
    for (int k = 0; k < TPS; ++k) consume(st, lds, qf, kr, vr);

    const f32x2 ml = {st.m, xhalf_sum(st.l)};
    u32x4 own[8];
#pragma unroll
    for (int e = 0; e < 8; ++e)
#pragma unroll
      for (int b = 0; b < 2; ++b) {
        const int g = e * 2 + b;
        own[e][2 * b] = pk_bf16(st.o[0][g], st.o[1][g]);
        own[e][2 * b + 1] = pk_bf16(st.o[2][g], st.o[3][g]);
      }
    const rsrc_t rs = mk_rsrc(ws, WS_BYTES);
    const int cwo = (bh * 4 + n) * GRP_BYTES + 64;
    const bool owner = split == n;
    bool ready = false;
    if (owner) {
#pragma unroll
      for (int e = 0; e < 8; ++e) *(u32x4*)(lds + L_OWN + e * 1024 + lane * 16) = own[e];
      *(f32x2*)(lds + L_OWN + PART_ML + lane * 8) = ml;
      asm volatile("s_waitcnt vmcnt(0)" ::: "memory");
#pragma nounroll
      for (int spin = 0; spin < SPIN_CAP; ++spin) {
        asm volatile("" ::: "memory");
        if ((unsigned)__builtin_amdgcn_readfirstlane(__builtin_amdgcn_raw_buffer_load_b32(rs, cwo, 0, AUX_SC1)) == ((unsigned)tag | 3u)) { ready = true; break; }
        __builtin_amdgcn_s_sleep(1);
      }
      if (lane == 0) *(int*)(lds + L_OWN + PART_BYTES) = ready;
    }
    if (!ready) {
      const int rec0 = (bh * 4 + n) * GRP_BYTES + 128;
      const int rec = rec0 + split * PART_BYTES;
#pragma unroll
      for (int e = 0; e < 8; ++e) __builtin_amdgcn_raw_buffer_store_b128(own[e], rs, lane * 16 + e * 1024, rec, AUX_SC1);
      __builtin_amdgcn_raw_buffer_store_b64(__builtin_bit_cast(u32x2, ml), rs, lane * 8 + PART_ML, rec, AUX_SC1);
      asm volatile("s_waitcnt vmcnt(0)" ::: "memory");
      u64 old = 0;
      if (lane == 0) old = __hip_atomic_fetch_add(cw, owner ? 8ull : 1ull, RLX_AGENT);
      const unsigned o4 = (unsigned)__builtin_amdgcn_readfirstlane((unsigned)old) & 15u;
      if (__builtin_expect(o4 == (owner ? 3u : 10u), 0)) {
        __builtin_amdgcn_fence(__ATOMIC_ACQUIRE, "agent");
        const int rp = rec0 + (split ^ 1) * PART_BYTES, ra = rec0 + ((split & 2) ^ 2) * PART_BYTES, rb = ra + PART_BYTES;
        const Wts w = merge_weights(ml, ld_ml(rs, lane, rp), ld_ml(rs, lane, ra), ld_ml(rs, lane, rb));
        char* op = (char*)out + (size_t)((bh * 4 + n) * SQ + c) * (DH * 2) + h * 32;
#pragma unroll
        for (int e = 0; e < 8; ++e)
          *(u32x4*)(op + (e >> 1) * 64 + (e & 1) * 16) =
              merge_piece(own[e], ld_piece(rs, lane, e, rp), ld_piece(rs, lane, e, ra), ld_piece(rs, lane, e, rb), w);
        if (lane == 0) { __hip_atomic_store(cw, tag | 15ull, RLX_AGENT); __hip_atomic_store(ew, epoch + 1, RLX_AGENT); }
      }
    }
    __syncthreads();
    if (__builtin_amdgcn_readfirstlane(*(const int*)(lds + L_OWN + PART_BYTES))) {
      const int rec0 = (bh * 4 + split) * GRP_BYTES + 128;
      const int rp = rec0 + (split ^ 1) * PART_BYTES, ra = rec0 + ((split & 2) ^ 2) * PART_BYTES, rb = ra + PART_BYTES;
      const f32x2 mp = ld_ml(rs, lane, rp), ma = ld_ml(rs, lane, ra), mb = ld_ml(rs, lane, rb);
      const int e0 = 2 * n;
      const u32x4 xp0 = ld_piece(rs, lane, e0, rp), xa0 = ld_piece(rs, lane, e0, ra), xb0 = ld_piece(rs, lane, e0, rb);
      const u32x4 xp1 = ld_piece(rs, lane, e0 + 1, rp), xa1 = ld_piece(rs, lane, e0 + 1, ra), xb1 = ld_piece(rs, lane, e0 + 1, rb);
      const f32x2 mo = *(const f32x2*)(lds + L_OWN + PART_ML + lane * 8);
      const u32x4 xo0 = *(const u32x4*)(lds + L_OWN + e0 * 1024 + lane * 16);
      const u32x4 xo1 = *(const u32x4*)(lds + L_OWN + e0 * 1024 + 1024 + lane * 16);
      SB();
      const Wts w = merge_weights(mo, mp, ma, mb);
      char* op = (char*)out + (size_t)((bh * 4 + split) * SQ + c) * (DH * 2) + h * 32 + n * 64;
      *(u32x4*)op = merge_piece(xo0, xp0, xa0, xb0, w);
      *(u32x4*)(op + 16) = merge_piece(xo1, xp1, xa1, xb1, w);
      if (owner && lane == 0) { __hip_atomic_store(cw, tag | 15ull, RLX_AGENT); __hip_atomic_store(ew, epoch + 1, RLX_AGENT); }
    }
  }
}

}

extern "C" void kernel_launch(void* const* d_in, const int* in_sizes, int n_in, void* d_out, int out_size,
                              void* d_ws, size_t ws_size, hipStream_t stream) {
  using namespace kvq;
  (void)in_sizes; (void)n_in; (void)out_size; (void)ws_size;
  (void)hipFuncSetAttribute((const void*)attn_kernel, hipFuncAttributeMaxDynamicSharedMemorySize, LDS_BYTES);
  hipLaunchKernelGGL(attn_kernel, dim3(NBH * NSPLIT), dim3(512), LDS_BYTES, stream,
                     (const __hip_bfloat16*)d_in[0], (const int*)d_in[1], (const int*)d_in[2],
                     (const __hip_bfloat16*)d_in[3], (const __hip_bfloat16*)d_in[4],
                     (const __hip_bfloat16*)d_in[5], (const __hip_bfloat16*)d_in[6],
                     (__hip_bfloat16*)d_out, (char*)d_ws);
}
```

```cpp
#define KVQ_WF 8
#define KVQ_LATE 1
#define KVQ_WEFF 6
#define KVQ_PSUB 8
#define KVQ_PACE 2560
#define KVQ_CREDIT 1000000
#define KVQ_TOUCH 3
#include <hip/hip_runtime.h>
#include <hip/hip_bf16.h>
#include <stdint.h>

namespace kvq {

constexpr int NB = 8, HQ = 32, HKV = 8, SQ = 32, SKV = 8192, DH = 128;
constexpr int TILE = 64;
constexpr int NGK = SKV / TILE;
constexpr int NSPLIT = 4;
constexpr int NBH = NB * HKV;
constexpr int TPS = NGK / NSPLIT;
constexpr int KPS = TPS * TILE;
#define KVQ_PACE0 1000
#define KVQ_FLIP 0
#define KVQ_TOUCHK 28
constexpr int WF = KVQ_WF;
constexpr int SPIN_CAP = 4096;

constexpr int KROW = 272;
constexpr int L_K = 0;
constexpr int L_V = TILE * KROW;
constexpr int L_KS = L_V + 16 * 1024;
constexpr int L_KZ = L_KS + TPS * DH * 2;
constexpr int L_VS = L_KZ + TPS * DH * 2;
constexpr int L_VZ = L_VS + KPS * 2;
constexpr int L_OWN = L_VZ + KPS * 2;
constexpr int L_Q = L_OWN + 8 * 1024 + 512 + 16;
constexpr int LDS_BYTES = L_Q + 4 * SQ * KROW;

constexpr int PART_ML = 8 * 1024;
constexpr int PART_BYTES = PART_ML + 512;
constexpr int GRP_BYTES = 128 + NSPLIT * PART_BYTES;
constexpr int WS_BYTES = NBH * 4 * GRP_BYTES;

constexpr float SOFTMAX_C = 0.08838834764831845f * 1.4426950408889634f;

typedef unsigned u32x4 __attribute__((ext_vector_type(4)));
typedef unsigned u32x2 __attribute__((ext_vector_type(2)));
typedef float f32x2 __attribute__((ext_vector_type(2)));
typedef float f32x16 __attribute__((ext_vector_type(16)));
typedef __bf16 bf16x8 __attribute__((ext_vector_type(8)));
typedef __bf16 bf16x2 __attribute__((ext_vector_type(2)));
typedef unsigned long long u64;
typedef __attribute__((address_space(1))) u64 gu64;
typedef __amdgpu_buffer_rsrc_t rsrc_t;
#define RLX_AGENT __ATOMIC_RELAXED, __HIP_MEMORY_SCOPE_AGENT
#define SB() __builtin_amdgcn_sched_barrier(0)
#define WG_BARRIER() do { SB(); __syncthreads(); SB(); } while (0)
constexpr int AUX_SC1 = 16, AUX_NT = 2;

__device__ __forceinline__ float bf_lo(unsigned w) { return __uint_as_float(w << 16); }
__device__ __forceinline__ float bf_hi(unsigned w) { return __uint_as_float(w & 0xffff0000u); }
__device__ __forceinline__ float i2f(unsigned x) { return (float)(int)x; }
__device__ __forceinline__ unsigned pk_bf16(float a, float b) {
  f32x2 v = {a, b};
  return __builtin_bit_cast(unsigned, __builtin_convertvector(v, bf16x2));
}
__device__ __forceinline__ float xhalf_max(float x) {
  auto r = __builtin_amdgcn_permlane32_swap(__float_as_uint(x), __float_as_uint(x), false, false);
  return fmaxf(__uint_as_float(r[0]), __uint_as_float(r[1]));
}
__device__ __forceinline__ float xhalf_sum(float x) {
  auto r = __builtin_amdgcn_permlane32_swap(__float_as_uint(x), __float_as_uint(x), false, false);
  return __uint_as_float(r[0]) + __uint_as_float(r[1]);
}
template <class T> __device__ __forceinline__ T ld_nt(const void* p) { return __builtin_nontemporal_load((const T*)p); }
__device__ __forceinline__ u64 uniform64(u64 x) {
  const unsigned lo = __builtin_amdgcn_readfirstlane((unsigned)x), hi = __builtin_amdgcn_readfirstlane((unsigned)(x >> 32));
  return ((u64)hi << 32) | lo;
}
__device__ __forceinline__ rsrc_t mk_rsrc(const void* p, int bytes) {
  return __builtin_amdgcn_make_buffer_rsrc((void*)p, 0, bytes, 0x00020000);
}

struct ProdCtx {
  rsrc_t rk, rv;
  unsigned koff, voff;
  char* lds;
  unsigned kw, vw;
  unsigned ksr, vsr;
  int flip;
};
#define KVQ_WEFF2 KVQ_WEFF
#define THROTTLE() asm volatile("s_waitcnt vmcnt(%0)" :: "n"(KVQ_WEFF - 1) : "memory")
#define THROTTLE2() asm volatile("s_waitcnt vmcnt(%0)" :: "n"(KVQ_WEFF2 - 1) : "memory")
struct Scales { u32x2 ks, kz; u32x4 vs, vz; };

__device__ __forceinline__ u32x4 ld_k(const ProdCtx& c, int t, int i) {
  return __builtin_amdgcn_raw_buffer_load_b128(c.rk, c.koff + (t ^ c.flip) * (TILE * DH * 4) + (i & 3) * 1024, (i >> 2) * 4096, AUX_NT);
}
__device__ __forceinline__ u32x4 ld_v(const ProdCtx& c, int t, int j) {
  return __builtin_amdgcn_raw_buffer_load_b128(c.rv, c.voff + (t ^ c.flip) * (TILE * DH * 4) + j * 512, 0, AUX_NT);
}

__device__ __forceinline__ void pace(unsigned& tn, unsigned step) {
  int lag;
  for (int i = 0;; ++i) { u64 now; asm volatile("s_memrealtime %0\n\ts_waitcnt lgkmcnt(0)" : "=s"(now) :: "memory");
    lag = (int)((unsigned)now * 10u - tn);
    if (lag >= 0 || i >= 64) break; __builtin_amdgcn_s_sleep(1); }
  tn += step + (unsigned)max(lag - KVQ_CREDIT, 0);
}
#define PACE(n) pace(tn, KVQ_PACE / (n))
__device__ __forceinline__ void produce(const ProdCtx& c, u32x4 (&rk)[8], Scales& sc, int k, unsigned& tn) {
  u32x4 rv[8];
  {
    const float s0 = bf_lo(sc.ks[0]), s1 = bf_hi(sc.ks[0]), s2 = bf_lo(sc.ks[1]), s3 = bf_hi(sc.ks[1]);
    const float c0 = -bf_lo(sc.kz[0]) * s0, c1 = -bf_hi(sc.kz[0]) * s1, c2 = -bf_lo(sc.kz[1]) * s2, c3 = -bf_hi(sc.kz[1]) * s3;
    SB();
#pragma unroll
    for (int t = 0; t < 8; ++t) {
      if (t % (16 / KVQ_PSUB) == 0) { PACE(KVQ_PSUB); SB(); }
      const u32x4 x = rk[t];
      u32x2 w;
      w[0] = pk_bf16(fmaf(i2f(x[0]), s0, c0), fmaf(i2f(x[1]), s1, c1));
      w[1] = pk_bf16(fmaf(i2f(x[2]), s2, c2), fmaf(i2f(x[3]), s3, c3));
      asm volatile("" : "+v"(w));
      SB();
      *(u32x2*)(c.lds + c.kw + t * (2 * KROW)) = w;
      SB();
      if (t < 7)
      { THROTTLE();
      if (t + WF < 8) rk[t + WF] = ld_k(c, k, t + WF); else rv[t + WF - 8] = ld_v(c, k, t + WF - 8); }
      SB();
    }
  }
  const u32x4 sw = sc.vs, zw = sc.vz;
  WG_BARRIER();
  THROTTLE(); rv[7] = ld_v(c, k, 7); SB();
  sc.ks = *(const u32x2*)(c.lds + L_KS + c.ksr + (((k + 1) ^ c.flip) & (TPS - 1)) * (DH * 2));
  sc.kz = *(const u32x2*)(c.lds + L_KZ + c.ksr + (((k + 1) ^ c.flip) & (TPS - 1)) * (DH * 2));
  sc.vs = *(const u32x4*)(c.lds + L_VS + c.vsr + (((k + 1) ^ c.flip) & (TPS - 1)) * (TILE * 2));
  sc.vz = *(const u32x4*)(c.lds + L_VZ + c.vsr + (((k + 1) ^ c.flip) & (TPS - 1)) * (TILE * 2));
  SB();
  {
    u32x4 f0, f1, f2, f3;
#pragma unroll
    for (int jj = 0; jj < 4; ++jj) {
      if (KVQ_PSUB == 16) { PACE(8); SB(); } else if (KVQ_PSUB >= 2 && (2 * jj) % (16 / KVQ_PSUB) == 0) { PACE(KVQ_PSUB); SB(); }
      const float sa = bf_lo(sw[jj]), sb = bf_hi(sw[jj]);
      const float ca = -bf_lo(zw[jj]) * sa, cb = -bf_hi(zw[jj]) * sb;
      const u32x4 xa = rv[2 * jj], xb = rv[2 * jj + 1];
      unsigned g0 = pk_bf16(fmaf(i2f(xa[0]), sa, ca), fmaf(i2f(xb[0]), sb, cb));
      unsigned g1 = pk_bf16(fmaf(i2f(xa[1]), sa, ca), fmaf(i2f(xb[1]), sb, cb));
      unsigned g2 = pk_bf16(fmaf(i2f(xa[2]), sa, ca), fmaf(i2f(xb[2]), sb, cb));
      unsigned g3 = pk_bf16(fmaf(i2f(xa[3]), sa, ca), fmaf(i2f(xb[3]), sb, cb));
      asm volatile("" : "+v"(g0), "+v"(g1), "+v"(g2), "+v"(g3));
      f0[jj] = g0; f1[jj] = g1; f2[jj] = g2; f3[jj] = g3;
      SB();
#pragma unroll
      for (int u = 2 * jj + WF; u < 2 * jj + WF + 2; ++u) {
        if (jj == 3) continue;
        THROTTLE2();
        if (u < 8) rv[u] = ld_v(c, k, u); else rk[u - 8] = ld_k(c, k + 1, u - 8);
      }
      SB();
    }
    char* vb = c.lds + c.vw;
    *(u32x4*)(vb + 0 * 1024) = f0;
    *(u32x4*)(vb + 1 * 1024) = f1;
    *(u32x4*)(vb + 2 * 1024) = f2;
    *(u32x4*)(vb + 3 * 1024) = f3;
  }
  WG_BARRIER();
  THROTTLE(); rk[6] = ld_k(c, k + 1, 6); THROTTLE(); rk[7] = ld_k(c, k + 1, 7); SB();
}

struct ConsState { f32x16 o[4]; float m, l; };

constexpr float RESC_THR = 8.0f;
__device__ __forceinline__ float block_max(const f32x16& s) {
  float a = fmaxf(fmaxf(s[0], s[1]), s[2]), b = fmaxf(fmaxf(s[3], s[4]), s[5]);
  a = fmaxf(fmaxf(a, s[6]), s[7]); b = fmaxf(fmaxf(b, s[8]), s[9]);
  a = fmaxf(fmaxf(a, s[10]), s[11]); b = fmaxf(fmaxf(b, s[12]), s[13]);
  a = fmaxf(fmaxf(a, s[14]), s[15]);
  return xhalf_max(fmaxf(a, b)) * SOFTMAX_C;
}
template <bool PEND>
__device__ __forceinline__ void guard(ConsState& st, float mx, f32x16& pend) {
  if (__builtin_expect(__builtin_amdgcn_ballot_w64(!(mx - st.m <= RESC_THR)) != 0ull, 0)) {
    const float mn = fmaxf(st.m, mx);
    const float alpha = __builtin_amdgcn_exp2f(st.m - mn);
    st.m = mn;
    st.l *= alpha;
#pragma unroll
    for (int i = 0; i < 4; ++i)
#pragma unroll
      for (int g = 0; g < 16; ++g) st.o[i][g] *= alpha;
    if (PEND) {
#pragma unroll
      for (int g = 0; g < 16; ++g) pend[g] *= alpha;
    }
  }
}
__device__ __forceinline__ bf16x8 ldsf(const char* p) { return *(const bf16x8*)p; }
__device__ __forceinline__ f32x16 mfma(bf16x8 a, bf16x8 b, f32x16 c) { return __builtin_amdgcn_mfma_f32_32x32x16_bf16(a, b, c, 0, 0, 0); }
__device__ __forceinline__ bf16x8 pack8(const f32x16& p, int o) {
  u32x4 w = {pk_bf16(p[o], p[o + 1]), pk_bf16(p[o + 2], p[o + 3]), pk_bf16(p[o + 4], p[o + 5]), pk_bf16(p[o + 6], p[o + 7])};
  return __builtin_bit_cast(bf16x8, w);
}
#define EXP2(S, g) do { S[g] = __builtin_amdgcn_exp2f(fmaf(S[g], SOFTMAX_C, mc)); S[(g) + 1] = __builtin_amdgcn_exp2f(fmaf(S[(g) + 1], SOFTMAX_C, mc)); } while (0)
#define SUM2(S, g) do { ls += S[g]; ls += S[(g) + 1]; } while (0)
#define PIN2(S, g) do { float e0_ = S[g], e1_ = S[(g) + 1]; asm volatile("" : "+v"(e0_), "+v"(e1_), "+v"(ls)); S[g] = e0_; S[(g) + 1] = e1_; } while (0)

__device__ __forceinline__ void consume(ConsState& st, const char* lds, const bf16x8 (&qf)[8], unsigned kr, unsigned vr) {
  f32x16 s0, s1;
#pragma unroll
  for (int g = 0; g < 16; ++g) { s0[g] = 0.f; s1[g] = 0.f; }
  bf16x8 fa[8], fb[8];
  WG_BARRIER();
#pragma unroll
  for (int s = 0; s < 8; ++s) fa[s] = ldsf(lds + kr + s * 32);
#pragma unroll
  for (int s = 0; s < 8; ++s) fb[s] = ldsf(lds + kr + 32 * KROW + s * 32);
  SB();
#pragma unroll
  for (int s = 0; s < 8; ++s) s0 = mfma(fa[s], qf[s], s0);
  SB();
  s1 = mfma(fb[0], qf[0], s1);
  SB();
  guard<false>(st, block_max(s0), s0);
  {
    const float mc = -st.m;
    float ls = st.l;
    SB();
#pragma unroll
    for (int s = 1; s < 8; ++s) {
      s1 = mfma(fb[s], qf[s], s1);
      EXP2(s0, 2 * (s - 1));
      if (s > 1) SUM2(s0, 2 * (s - 2));
      PIN2(s0, 2 * (s - 1));
      SB();
    }
    EXP2(s0, 14); SUM2(s0, 12); SUM2(s0, 14);
    st.l = ls;
    SB();
  }
  guard<true>(st, block_max(s1), s0);
  const float mc = -st.m;
  float ls = st.l;
  bf16x8 p0 = pack8(s0, 0), p1 = pack8(s0, 8);
  EXP2(s1, 0); PIN2(s1, 0);
  asm volatile("" : "+v"(p0), "+v"(p1));
  WG_BARRIER();
#pragma unroll
  for (int s = 0; s < 8; ++s) fa[s] = ldsf(lds + vr + s * 1024);
#pragma unroll
  for (int s = 0; s < 8; ++s) fb[s] = ldsf(lds + vr + (8 + s) * 1024);
  EXP2(s1, 2); SUM2(s1, 0); PIN2(s1, 2);
  SB();
  bf16x8 p2;
#pragma unroll
  for (int i = 0; i < 8; ++i) {
    st.o[i & 3] = mfma(fa[i], i < 4 ? p0 : p1, st.o[i & 3]);
    if (i < 6) { EXP2(s1, 2 * i + 4); SUM2(s1, 2 * i + 2); PIN2(s1, 2 * i + 4); }
    if (i == 6) { p2 = pack8(s1, 0); SUM2(s1, 14); asm volatile("" : "+v"(p2), "+v"(ls)); }
    SB();
  }
  st.l = ls;
  st.o[0] = mfma(fb[0], p2, st.o[0]);
  const bf16x8 p3 = pack8(s1, 8);
  SB();
#pragma unroll
  for (int i = 1; i < 8; ++i) st.o[i & 3] = mfma(fb[i], i < 4 ? p2 : p3, st.o[i & 3]);
}

__device__ __forceinline__ float merge4(float a, float wa, float b, float wb, float c, float wc, float d, float wd) {
#pragma clang fp contract(off)
  const float p = a * wa, q = b * wb, r = c * wc, s = d * wd;
  return (p + q) + (r + s);
}
struct Wts { float o, p, a, b; };
__device__ __forceinline__ Wts merge_weights(f32x2 mo, f32x2 mp, f32x2 ma, f32x2 mb) {
  const float M = fmaxf(fmaxf(mo[0], mp[0]), fmaxf(ma[0], mb[0]));
  Wts w;
  w.o = __builtin_amdgcn_exp2f(mo[0] - M); w.p = __builtin_amdgcn_exp2f(mp[0] - M);
  w.a = __builtin_amdgcn_exp2f(ma[0] - M); w.b = __builtin_amdgcn_exp2f(mb[0] - M);
  const float inv = 1.f / merge4(mo[1], w.o, mp[1], w.p, ma[1], w.a, mb[1], w.b);
  w.o *= inv; w.p *= inv; w.a *= inv; w.b *= inv;
  return w;
}
__device__ __forceinline__ u32x4 merge_piece(u32x4 xo, u32x4 xp, u32x4 xa, u32x4 xb, const Wts& w) {
  u32x4 r;
#pragma unroll
  for (int j = 0; j < 4; ++j)
    r[j] = pk_bf16(merge4(bf_lo(xo[j]), w.o, bf_lo(xp[j]), w.p, bf_lo(xa[j]), w.a, bf_lo(xb[j]), w.b),
                   merge4(bf_hi(xo[j]), w.o, bf_hi(xp[j]), w.p, bf_hi(xa[j]), w.a, bf_hi(xb[j]), w.b));
  return r;
}
__device__ __forceinline__ f32x2 ld_ml(rsrc_t rs, int lane, int rec) {
  return __builtin_bit_cast(f32x2, __builtin_amdgcn_raw_buffer_load_b64(rs, lane * 8 + PART_ML, rec, AUX_SC1));
}
__device__ __forceinline__ u32x4 ld_piece(rsrc_t rs, int lane, int e, int rec) {
  return __builtin_amdgcn_raw_buffer_load_b128(rs, lane * 16 + e * 1024, rec, AUX_SC1);
}

__global__ void __launch_bounds__(512, 2)
attn_kernel(const __hip_bfloat16* __restrict__ q, const int* __restrict__ qk, const int* __restrict__ qv,
            const __hip_bfloat16* __restrict__ ksc, const __hip_bfloat16* __restrict__ kzr,
            const __hip_bfloat16* __restrict__ vsc, const __hip_bfloat16* __restrict__ vzr,
            __hip_bfloat16* __restrict__ out, char* ws) {
  extern __shared__ __attribute__((aligned(16))) char lds[];
  const int tid = threadIdx.x, lane = tid & 63;
  const int wave = __builtin_amdgcn_readfirstlane(tid >> 6);
  const int bh = blockIdx.x & (NBH - 1), split = blockIdx.x >> 6;
  const int c = lane & 31, h = lane >> 5;
  const size_t kso = ((size_t)bh * NGK + (size_t)split * TPS) * DH * 2;
  const size_t vso = ((size_t)bh * SKV + (size_t)split * KPS) * 2;

  if (wave < 4) {
    __builtin_amdgcn_s_setprio(3);
    ProdCtx pc;
    const size_t base = ((size_t)bh * SKV + (size_t)split * KPS) * (DH * 4);
    pc.rk = mk_rsrc((const char*)qk + base, KPS * DH * 4);
    pc.rv = mk_rsrc((const char*)qv + base, KPS * DH * 4);
    pc.koff = wave * 8192 + lane * 16;
    pc.voff = wave * 8192 + h * 4096 + c * 16;
    pc.lds = lds;
    pc.kw = L_K + (16 * wave + h) * KROW + c * 8;
    pc.vw = L_V + wave * 4096 + lane * 16;
    pc.ksr = c * 8;
    pc.vsr = (16 * wave + 8 * h) * 2;
    pc.flip = (KVQ_FLIP && (split & 1)) ? TPS - 1 : 0;
    Scales sc;
    sc.ks = ld_nt<u32x2>((const char*)ksc + kso + pc.ksr + pc.flip * (DH * 2));
    sc.kz = ld_nt<u32x2>((const char*)kzr + kso + pc.ksr + pc.flip * (DH * 2));
    sc.vs = ld_nt<u32x4>((const char*)vsc + vso + pc.vsr + pc.flip * (TILE * 2));
    sc.vz = ld_nt<u32x4>((const char*)vzr + vso + pc.vsr + pc.flip * (TILE * 2));
    SB();
    u32x4 rk[8];
#pragma unroll
    for (int i = 0; i < WF; ++i) rk[i] = ld_k(pc, 0, i);
    SB();
    unsigned tn = 0;
    { u64 t0; asm volatile("s_memrealtime %0\n\ts_waitcnt lgkmcnt(0)" : "=s"(t0) :: "memory"); tn = (unsigned)t0 * 10u + KVQ_PACE0; }
    for (int k = 0; k < TPS; ++k) produce(pc, rk, sc, k, tn);
    asm volatile("s_waitcnt vmcnt(0)" ::: "memory");
  } else {
    const int n = wave - 4;
    const int ct = tid - 256;
    gu64* const ew = (gu64*)(ws + (bh * 4 + n) * GRP_BYTES);
    gu64* const cw = ew + 8;
    const u32x4 a0 = ld_nt<u32x4>((const char*)ksc + kso + ct * 16), b0 = ld_nt<u32x4>((const char*)kzr + kso + ct * 16);
    const u32x4 c0 = ld_nt<u32x4>((const char*)vsc + vso + ct * 16), d0 = ld_nt<u32x4>((const char*)vzr + vso + ct * 16);
    const u32x4 a1 = ld_nt<u32x4>((const char*)ksc + kso + 4096 + ct * 16), b1 = ld_nt<u32x4>((const char*)kzr + kso + 4096 + ct * 16);
    SB();
    u32x4 qraw[8];
    {
      const char* qp = (const char*)q + ((size_t)bh * 4 + n) * (SQ * DH * 2) + lane * 16;
#pragma unroll
      for (int i = 0; i < 8; ++i) qraw[i] = ld_nt<u32x4>(qp + i * 1024);
    }
    SB();
    u64 e0v = __hip_atomic_load(ew, RLX_AGENT);
    u64 c0v = __hip_atomic_load(cw, RLX_AGENT);
    SB();
    *(u32x4*)(lds + L_KS + ct * 16) = a0; *(u32x4*)(lds + L_KZ + ct * 16) = b0;
    *(u32x4*)(lds + L_VS + ct * 16) = c0; *(u32x4*)(lds + L_VZ + ct * 16) = d0;
    *(u32x4*)(lds + L_KS + 4096 + ct * 16) = a1; *(u32x4*)(lds + L_KZ + 4096 + ct * 16) = b1;
    bf16x8 qf[8];
    {
      char* qs = lds + L_Q + n * (SQ * KROW);
#pragma unroll
      for (int i = 0; i < 8; ++i) *(u32x4*)(qs + (4 * i + (lane >> 4)) * KROW + (lane & 15) * 16) = qraw[i];
      __builtin_amdgcn_fence(__ATOMIC_RELEASE, "wavefront");
      __builtin_amdgcn_wave_barrier();
      __builtin_amdgcn_fence(__ATOMIC_ACQUIRE, "wavefront");
#pragma unroll
      for (int s = 0; s < 8; ++s) qf[s] = *(const bf16x8*)(qs + c * KROW + h * 16 + s * 32);
    }
    const u64 epoch = uniform64(e0v);
    const u64 tag = ((epoch + 1) * 0x9E3779B97F4A7C15ull) & ~15ull;
    {
      u64 cur = uniform64(c0v);
      const bool mine = (cur & ~15ull) == tag && (cur & 15ull) != 15ull;
      if (!mine && lane == 0) __hip_atomic_compare_exchange_strong(cw, &cur, tag, __ATOMIC_RELAXED, RLX_AGENT);
    }

    const int pr = (c & 0x13) | ((c & 4) << 1) | ((c & 8) >> 1);
    const unsigned kr = L_K + pr * KROW + h * 16;
    const unsigned vr = L_V + lane * 16;
    ConsState st;
#pragma unroll
    for (int i = 0; i < 4; ++i)
#pragma unroll
      for (int g = 0; g < 16; ++g) st.o[i][g] = 0.f;
    st.m = -__builtin_inff(); st.l = 0.f;
    for (int k = 0; k < TPS; ++k) {
      if (k == KVQ_TOUCHK) {
        const rsrc_t rw = mk_rsrc(ws, WS_BYTES), rq = mk_rsrc(out, NB * HQ * SQ * DH * 2);
        unsigned x0 = 0, x1 = 0, x2 = 0;
        if (KVQ_TOUCH & 1) {
          x0 = __builtin_amdgcn_raw_buffer_load_b32(rw, lane < 9 ? (bh * 4 + n) * GRP_BYTES + lane * 4096 : 0x7fffff00, 0, AUX_SC1);
          x1 = __builtin_amdgcn_raw_buffer_load_b32(rw, lane < 9 ? (bh * 4 + split) * GRP_BYTES + lane * 4096 : 0x7fffff00, 0, AUX_SC1);
          x2 = __builtin_amdgcn_raw_buffer_load_b32(rq, lane < 2 ? (bh * 4 + split) * (SQ * DH * 2) + lane * 4096 : 0x7fffff00, 0, AUX_NT);
        }
        u32x4 pf = {0, 0, 0, 0};
        if (KVQ_TOUCH & 2) {
          u64 pc; int len;
          asm volatile("s_getpc_b64 %0\n.Lkvq_code_lo:\n\ts_mov_b32 %1, .Lkvq_code_hi-.Lkvq_code_lo\n\ts_max_i32 %1, %1, 0\n\ts_and_b32 %1, %1, 0x7ffffff0"
                       : "=s"(pc), "=s"(len) :: "scc");
          const rsrc_t rc = mk_rsrc((void*)(pc & ~15ull), len);
          pf = __builtin_amdgcn_raw_buffer_load_b128(rc, ct * 16, 0, 0);
          u32x4 pg = __builtin_amdgcn_raw_buffer_load_b128(rc, ct * 16 + 4096, 0, 0);
          pf ^= pg;
        }
        asm volatile("" :: "v"(x0), "v"(x1), "v"(x2), "v"(pf));
      }
      consume(st, lds, qf, kr, vr);
    }

    const f32x2 ml = {st.m, xhalf_sum(st.l)};
    u32x4 own[8];
#pragma unroll
    for (int e = 0; e < 8; ++e)
#pragma unroll
      for (int b = 0; b < 2; ++b) {
        const int g = e * 2 + b;
        own[e][2 * b] = pk_bf16(st.o[0][g], st.o[1][g]);
        own[e][2 * b + 1] = pk_bf16(st.o[2][g], st.o[3][g]);
      }
    const rsrc_t rs = mk_rsrc(ws, WS_BYTES);
    const int cwo = (bh * 4 + n) * GRP_BYTES + 64;
    const bool owner = split == n;
    bool ready = false;
    if (owner) {
#pragma unroll
      for (int e = 0; e < 8; ++e) *(u32x4*)(lds + L_OWN + e * 1024 + lane * 16) = own[e];
      *(f32x2*)(lds + L_OWN + PART_ML + lane * 8) = ml;
      asm volatile("s_waitcnt vmcnt(0)" ::: "memory");
#pragma nounroll
      for (int spin = 0; spin < SPIN_CAP; ++spin) {
        asm volatile("" ::: "memory");
        if ((unsigned)__builtin_amdgcn_readfirstlane(__builtin_amdgcn_raw_buffer_load_b32(rs, cwo, 0, AUX_SC1)) == ((unsigned)tag | 3u)) { ready = true; break; }
        __builtin_amdgcn_s_sleep(1);
      }
      if (lane == 0) *(int*)(lds + L_OWN + PART_BYTES) = ready;
    }
    if (!ready) {
      const int rec0 = (bh * 4 + n) * GRP_BYTES + 128;
      const int rec = rec0 + split * PART_BYTES;
#pragma unroll
      for (int e = 0; e < 8; ++e) __builtin_amdgcn_raw_buffer_store_b128(own[e], rs, lane * 16 + e * 1024, rec, AUX_SC1);
      __builtin_amdgcn_raw_buffer_store_b64(__builtin_bit_cast(u32x2, ml), rs, lane * 8 + PART_ML, rec, AUX_SC1);
      asm volatile("s_waitcnt vmcnt(0)" ::: "memory");
      u64 old = 0;
      if (lane == 0) old = __hip_atomic_fetch_add(cw, owner ? 8ull : 1ull, RLX_AGENT);
      const unsigned o4 = (unsigned)__builtin_amdgcn_readfirstlane((unsigned)old) & 15u;
      if (__builtin_expect(o4 == (owner ? 3u : 10u), 0)) {
        __builtin_amdgcn_fence(__ATOMIC_ACQUIRE, "agent");
        const int rp = rec0 + (split ^ 1) * PART_BYTES, ra = rec0 + ((split & 2) ^ 2) * PART_BYTES, rb = ra + PART_BYTES;
        const Wts w = merge_weights(ml, ld_ml(rs, lane, rp), ld_ml(rs, lane, ra), ld_ml(rs, lane, rb));
        char* op = (char*)out + (size_t)((bh * 4 + n) * SQ + c) * (DH * 2) + h * 32;
#pragma unroll
        for (int e = 0; e < 8; ++e)
          *(u32x4*)(op + (e >> 1) * 64 + (e & 1) * 16) =
              merge_piece(own[e], ld_piece(rs, lane, e, rp), ld_piece(rs, lane, e, ra), ld_piece(rs, lane, e, rb), w);
        if (lane == 0) { __hip_atomic_store(cw, tag | 15ull, RLX_AGENT); __hip_atomic_store(ew, epoch + 1, RLX_AGENT); }
      }
    }
    __syncthreads();
    if (__builtin_amdgcn_readfirstlane(*(const int*)(lds + L_OWN + PART_BYTES))) {
      const int rec0 = (bh * 4 + split) * GRP_BYTES + 128;
      const int rp = rec0 + (split ^ 1) * PART_BYTES, ra = rec0 + ((split & 2) ^ 2) * PART_BYTES, rb = ra + PART_BYTES;
      const f32x2 mp = ld_ml(rs, lane, rp), ma = ld_ml(rs, lane, ra), mb = ld_ml(rs, lane, rb);
      const int e0 = 2 * n;
      const u32x4 xp0 = ld_piece(rs, lane, e0, rp), xa0 = ld_piece(rs, lane, e0, ra), xb0 = ld_piece(rs, lane, e0, rb);
      const u32x4 xp1 = ld_piece(rs, lane, e0 + 1, rp), xa1 = ld_piece(rs, lane, e0 + 1, ra), xb1 = ld_piece(rs, lane, e0 + 1, rb);
      const f32x2 mo = *(const f32x2*)(lds + L_OWN + PART_ML + lane * 8);
      const u32x4 xo0 = *(const u32x4*)(lds + L_OWN + e0 * 1024 + lane * 16);
      const u32x4 xo1 = *(const u32x4*)(lds + L_OWN + e0 * 1024 + 1024 + lane * 16);
      SB();
      const Wts w = merge_weights(mo, mp, ma, mb);
      char* op = (char*)out + (size_t)((bh * 4 + split) * SQ + c) * (DH * 2) + h * 32 + n * 64;
      *(u32x4*)op = merge_piece(xo0, xp0, xa0, xb0, w);
      *(u32x4*)(op + 16) = merge_piece(xo1, xp1, xa1, xb1, w);
      if (owner && lane == 0) { __hip_atomic_store(cw, tag | 15ull, RLX_AGENT); __hip_atomic_store(ew, epoch + 1, RLX_AGENT); }
      asm volatile(".Lkvq_code_hi:");
    }
  }
}

}

extern "C" void kernel_launch(void* const* d_in, const int* in_sizes, int n_in, void* d_out, int out_size,
                              void* d_ws, size_t ws_size, hipStream_t stream) {
  using namespace kvq;
  (void)in_sizes; (void)n_in; (void)out_size; (void)ws_size;
  (void)hipFuncSetAttribute((const void*)attn_kernel, hipFuncAttributeMaxDynamicSharedMemorySize, LDS_BYTES);
  hipLaunchKernelGGL(attn_kernel, dim3(NBH * NSPLIT), dim3(512), LDS_BYTES, stream,
                     (const __hip_bfloat16*)d_in[0], (const int*)d_in[1], (const int*)d_in[2],
                     (const __hip_bfloat16*)d_in[3], (const __hip_bfloat16*)d_in[4],
                     (const __hip_bfloat16*)d_in[5], (const __hip_bfloat16*)d_in[6],
                     (__hip_bfloat16*)d_out, (char*)d_ws);
}
```

```cpp
#include <hip/hip_runtime.h>
#include <hip/hip_bf16.h>
#include <stdint.h>

namespace kvq {

constexpr int NB = 8, HQ = 32, HKV = 8, SQ = 32, SKV = 8192, DH = 128;
constexpr int TILE = 64;
constexpr int NGK = SKV / TILE;
constexpr int NSPLIT = 4;
constexpr int NBH = NB * HKV;
constexpr int TPS = NGK / NSPLIT;
constexpr int KPS = TPS * TILE;
constexpr int WEFF = 6;
constexpr unsigned PACE_NS = 2232 / 8;
constexpr int CREDIT_NS = 400, PACE0_NS = 500;
constexpr int SPIN_CAP = 4096;

constexpr int KROW = 272;
constexpr int L_K = 0;
constexpr int L_V = TILE * KROW;
constexpr int L_KS = L_V + 16 * 1024;
constexpr int L_KZ = L_KS + TPS * DH * 2;
constexpr int L_VS = L_KZ + TPS * DH * 2;
constexpr int L_VZ = L_VS + KPS * 2;
constexpr int L_OWN = L_VZ + KPS * 2;
constexpr int L_Q = L_OWN + 8 * 1024 + 512 + 16;
constexpr int LDS_BYTES = L_Q + 4 * SQ * KROW;

constexpr int PART_ML = 8 * 1024;
constexpr int PART_BYTES = PART_ML + 512;
constexpr int GRP_BYTES = 128 + NSPLIT * PART_BYTES;
constexpr int WS_BYTES = NBH * 4 * GRP_BYTES;

constexpr float SOFTMAX_C = 0.08838834764831845f * 1.4426950408889634f;

typedef unsigned u32x4 __attribute__((ext_vector_type(4)));
typedef unsigned u32x2 __attribute__((ext_vector_type(2)));
typedef float f32x2 __attribute__((ext_vector_type(2)));
typedef float f32x16 __attribute__((ext_vector_type(16)));
typedef __bf16 bf16x8 __attribute__((ext_vector_type(8)));
typedef __bf16 bf16x2 __attribute__((ext_vector_type(2)));
typedef unsigned long long u64;
typedef __attribute__((address_space(1))) u64 gu64;
typedef __amdgpu_buffer_rsrc_t rsrc_t;
#define RLX_AGENT __ATOMIC_RELAXED, __HIP_MEMORY_SCOPE_AGENT
#define SB() __builtin_amdgcn_sched_barrier(0)
#define WG_BARRIER() do { SB(); __syncthreads(); SB(); } while (0)
constexpr int AUX_SC1 = 16, AUX_NT = 2;

__device__ __forceinline__ float bf_lo(unsigned w) { return __uint_as_float(w << 16); }
__device__ __forceinline__ float bf_hi(unsigned w) { return __uint_as_float(w & 0xffff0000u); }
__device__ __forceinline__ float i2f(unsigned x) { return (float)(int)x; }
__device__ __forceinline__ unsigned pk_bf16(float a, float b) {
  f32x2 v = {a, b};
  return __builtin_bit_cast(unsigned, __builtin_convertvector(v, bf16x2));
}
__device__ __forceinline__ float xhalf_max(float x) {
  auto r = __builtin_amdgcn_permlane32_swap(__float_as_uint(x), __float_as_uint(x), false, false);
  return fmaxf(__uint_as_float(r[0]), __uint_as_float(r[1]));
}
__device__ __forceinline__ float xhalf_sum(float x) {
  auto r = __builtin_amdgcn_permlane32_swap(__float_as_uint(x), __float_as_uint(x), false, false);
  return __uint_as_float(r[0]) + __uint_as_float(r[1]);
}
template <class T> __device__ __forceinline__ T ld_nt(const void* p) { return __builtin_nontemporal_load((const T*)p); }
__device__ __forceinline__ u64 uniform64(u64 x) {
  const unsigned lo = __builtin_amdgcn_readfirstlane((unsigned)x), hi = __builtin_amdgcn_readfirstlane((unsigned)(x >> 32));
  return ((u64)hi << 32) | lo;
}
__device__ __forceinline__ rsrc_t mk_rsrc(const void* p, int bytes) {
  return __builtin_amdgcn_make_buffer_rsrc((void*)p, 0, bytes, 0x00020000);
}

struct ProdCtx {
  rsrc_t rk, rv;
  unsigned koff, voff;
  char* lds;
  unsigned kw, vw;
  unsigned ksr, vsr;
};
struct Scales { u32x2 ks, kz; u32x4 vs, vz; };

__device__ __forceinline__ u32x4 ld_k(const ProdCtx& c, int t, int i) {
  return __builtin_amdgcn_raw_buffer_load_b128(c.rk, c.koff + t * (TILE * DH * 4) + (i & 3) * 1024, (i >> 2) * 4096, AUX_NT);
}
__device__ __forceinline__ u32x4 ld_v(const ProdCtx& c, int t, int j) {
  return __builtin_amdgcn_raw_buffer_load_b128(c.rv, c.voff + t * (TILE * DH * 4) + j * 512, 0, AUX_NT);
}

__device__ __forceinline__ unsigned now_ns() {
  u64 t;
  asm volatile("s_memrealtime %0\n\ts_waitcnt lgkmcnt(0)" : "=s"(t) :: "memory");
  return (unsigned)t * 10u;
}
__device__ __forceinline__ void pace(unsigned& tn) {
  int lag;
#pragma nounroll
  for (int i = 0;; ++i) {
    lag = (int)(now_ns() - tn);
    if (lag >= 0 || i >= 64) break;
    __builtin_amdgcn_s_sleep(1);
  }
  tn += PACE_NS + (unsigned)max(lag - CREDIT_NS, 0);
  SB();
}
#define THROTTLE() asm volatile("s_waitcnt vmcnt(%0)" :: "n"(WEFF - 1) : "memory")

__device__ __forceinline__ void produce(const ProdCtx& c, u32x4 (&rk)[8], Scales& sc, int k, unsigned& tn) {
  u32x4 rv[8];
  {
    const float s0 = bf_lo(sc.ks[0]), s1 = bf_hi(sc.ks[0]), s2 = bf_lo(sc.ks[1]), s3 = bf_hi(sc.ks[1]);
    const float c0 = -bf_lo(sc.kz[0]) * s0, c1 = -bf_hi(sc.kz[0]) * s1, c2 = -bf_lo(sc.kz[1]) * s2, c3 = -bf_hi(sc.kz[1]) * s3;
    SB();
#pragma unroll
    for (int t = 0; t < 8; ++t) {
      if ((t & 1) == 0) pace(tn);
      const u32x4 x = rk[t];
      u32x2 w;
      w[0] = pk_bf16(fmaf(i2f(x[0]), s0, c0), fmaf(i2f(x[1]), s1, c1));
      w[1] = pk_bf16(fmaf(i2f(x[2]), s2, c2), fmaf(i2f(x[3]), s3, c3));
      asm volatile("" : "+v"(w));
      SB();
      *(u32x2*)(c.lds + c.kw + t * (2 * KROW)) = w;
      SB();
      if (t < 7) { THROTTLE(); rv[t] = ld_v(c, k, t); }
      SB();
    }
  }
  const u32x4 sw = sc.vs, zw = sc.vz;
  WG_BARRIER();
  THROTTLE(); rv[7] = ld_v(c, k, 7);
  SB();
  sc.ks = *(const u32x2*)(c.lds + L_KS + c.ksr + (k + 1) * (DH * 2));
  sc.kz = *(const u32x2*)(c.lds + L_KZ + c.ksr + (k + 1) * (DH * 2));
  sc.vs = *(const u32x4*)(c.lds + L_VS + c.vsr + (k + 1) * (TILE * 2));
  sc.vz = *(const u32x4*)(c.lds + L_VZ + c.vsr + (k + 1) * (TILE * 2));
  SB();
  {
    u32x4 f0, f1, f2, f3;
#pragma unroll
    for (int jj = 0; jj < 4; ++jj) {
      if (k + 1 < TPS) pace(tn);
      const float sa = bf_lo(sw[jj]), sb = bf_hi(sw[jj]);
      const float ca = -bf_lo(zw[jj]) * sa, cb = -bf_hi(zw[jj]) * sb;
      const u32x4 xa = rv[2 * jj], xb = rv[2 * jj + 1];
      unsigned g0 = pk_bf16(fmaf(i2f(xa[0]), sa, ca), fmaf(i2f(xb[0]), sb, cb));
      unsigned g1 = pk_bf16(fmaf(i2f(xa[1]), sa, ca), fmaf(i2f(xb[1]), sb, cb));
      unsigned g2 = pk_bf16(fmaf(i2f(xa[2]), sa, ca), fmaf(i2f(xb[2]), sb, cb));
      unsigned g3 = pk_bf16(fmaf(i2f(xa[3]), sa, ca), fmaf(i2f(xb[3]), sb, cb));
      asm volatile("" : "+v"(g0), "+v"(g1), "+v"(g2), "+v"(g3));
      f0[jj] = g0; f1[jj] = g1; f2[jj] = g2; f3[jj] = g3;
      SB();
      if (jj < 3) {
        THROTTLE(); rk[2 * jj] = ld_k(c, k + 1, 2 * jj);
        THROTTLE(); rk[2 * jj + 1] = ld_k(c, k + 1, 2 * jj + 1);
      }
      SB();
    }
    char* vb = c.lds + c.vw;
    *(u32x4*)(vb + 0 * 1024) = f0;
    *(u32x4*)(vb + 1 * 1024) = f1;
    *(u32x4*)(vb + 2 * 1024) = f2;
    *(u32x4*)(vb + 3 * 1024) = f3;
  }
  WG_BARRIER();
  THROTTLE(); rk[6] = ld_k(c, k + 1, 6);
  THROTTLE(); rk[7] = ld_k(c, k + 1, 7);
  SB();
}

struct ConsState { f32x16 o[4]; float m, l; };

constexpr float RESC_THR = 8.0f;
__device__ __forceinline__ float block_max(const f32x16& s) {
  float a = fmaxf(fmaxf(s[0], s[1]), s[2]), b = fmaxf(fmaxf(s[3], s[4]), s[5]);
  a = fmaxf(fmaxf(a, s[6]), s[7]); b = fmaxf(fmaxf(b, s[8]), s[9]);
  a = fmaxf(fmaxf(a, s[10]), s[11]); b = fmaxf(fmaxf(b, s[12]), s[13]);
  a = fmaxf(fmaxf(a, s[14]), s[15]);
  return xhalf_max(fmaxf(a, b)) * SOFTMAX_C;
}
template <bool PEND>
__device__ __forceinline__ void guard(ConsState& st, float mx, f32x16& pend) {
  if (__builtin_expect(__builtin_amdgcn_ballot_w64(!(mx - st.m <= RESC_THR)) != 0ull, 0)) {
    const float mn = fmaxf(st.m, mx);
    const float alpha = __builtin_amdgcn_exp2f(st.m - mn);
    st.m = mn;
    st.l *= alpha;
#pragma unroll
    for (int i = 0; i < 4; ++i)
#pragma unroll
      for (int g = 0; g < 16; ++g) st.o[i][g] *= alpha;
    if (PEND) {
#pragma unroll
      for (int g = 0; g < 16; ++g) pend[g] *= alpha;
    }
  }
}
__device__ __forceinline__ bf16x8 ldsf(const char* p) { return *(const bf16x8*)p; }
__device__ __forceinline__ f32x16 mfma(bf16x8 a, bf16x8 b, f32x16 c) { return __builtin_amdgcn_mfma_f32_32x32x16_bf16(a, b, c, 0, 0, 0); }
__device__ __forceinline__ bf16x8 pack8(const f32x16& p, int o) {
  u32x4 w = {pk_bf16(p[o], p[o + 1]), pk_bf16(p[o + 2], p[o + 3]), pk_bf16(p[o + 4], p[o + 5]), pk_bf16(p[o + 6], p[o + 7])};
  return __builtin_bit_cast(bf16x8, w);
}
#define EXP2(S, g) do { S[g] = __builtin_amdgcn_exp2f(fmaf(S[g], SOFTMAX_C, mc)); S[(g) + 1] = __builtin_amdgcn_exp2f(fmaf(S[(g) + 1], SOFTMAX_C, mc)); } while (0)
#define SUM2(S, g) do { ls += S[g]; ls += S[(g) + 1]; } while (0)
#define PIN2(S, g) do { float e0_ = S[g], e1_ = S[(g) + 1]; asm volatile("" : "+v"(e0_), "+v"(e1_), "+v"(ls)); S[g] = e0_; S[(g) + 1] = e1_; } while (0)

__device__ __forceinline__ void consume(ConsState& st, const char* lds, const bf16x8 (&qf)[8], unsigned kr, unsigned vr) {
  f32x16 s0, s1;
#pragma unroll
  for (int g = 0; g < 16; ++g) { s0[g] = 0.f; s1[g] = 0.f; }
  bf16x8 fa[8], fb[8];
  WG_BARRIER();
#pragma unroll
  for (int s = 0; s < 8; ++s) fa[s] = ldsf(lds + kr + s * 32);
#pragma unroll
  for (int s = 0; s < 8; ++s) fb[s] = ldsf(lds + kr + 32 * KROW + s * 32);
  SB();
#pragma unroll
  for (int s = 0; s < 8; ++s) s0 = mfma(fa[s], qf[s], s0);
  SB();
  s1 = mfma(fb[0], qf[0], s1);
  SB();
  guard<false>(st, block_max(s0), s0);
  {
    const float mc = -st.m;
    float ls = st.l;
    SB();
#pragma unroll
    for (int s = 1; s < 8; ++s) {
      s1 = mfma(fb[s], qf[s], s1);
      EXP2(s0, 2 * (s - 1));
      if (s > 1) SUM2(s0, 2 * (s - 2));
      PIN2(s0, 2 * (s - 1));
      SB();
    }
    EXP2(s0, 14); SUM2(s0, 12); SUM2(s0, 14);
    st.l = ls;
    SB();
  }
  guard<true>(st, block_max(s1), s0);
  const float mc = -st.m;
  float ls = st.l;
  bf16x8 p0 = pack8(s0, 0), p1 = pack8(s0, 8);
  EXP2(s1, 0); PIN2(s1, 0);
  asm volatile("" : "+v"(p0), "+v"(p1));
  WG_BARRIER();
#pragma unroll
  for (int s = 0; s < 8; ++s) fa[s] = ldsf(lds + vr + s * 1024);
#pragma unroll
  for (int s = 0; s < 8; ++s) fb[s] = ldsf(lds + vr + (8 + s) * 1024);
  EXP2(s1, 2); SUM2(s1, 0); PIN2(s1, 2);
  SB();
  bf16x8 p2;
#pragma unroll
  for (int i = 0; i < 8; ++i) {
    st.o[i & 3] = mfma(fa[i], i < 4 ? p0 : p1, st.o[i & 3]);
    if (i < 6) { EXP2(s1, 2 * i + 4); SUM2(s1, 2 * i + 2); PIN2(s1, 2 * i + 4); }
    if (i == 6) { p2 = pack8(s1, 0); SUM2(s1, 14); asm volatile("" : "+v"(p2), "+v"(ls)); }
    SB();
  }
  st.l = ls;
  st.o[0] = mfma(fb[0], p2, st.o[0]);
  const bf16x8 p3 = pack8(s1, 8);
  SB();
#pragma unroll
  for (int i = 1; i < 8; ++i) st.o[i & 3] = mfma(fb[i], i < 4 ? p2 : p3, st.o[i & 3]);
}

__device__ __forceinline__ float merge4(float a, float wa, float b, float wb, float c, float wc, float d, float wd) {
#pragma clang fp contract(off)
  const float p = a * wa, q = b * wb, r = c * wc, s = d * wd;
  return (p + q) + (r + s);
}
struct Wts { float o, p, a, b; };
__device__ __forceinline__ Wts merge_weights(f32x2 mo, f32x2 mp, f32x2 ma, f32x2 mb) {
  const float M = fmaxf(fmaxf(mo[0], mp[0]), fmaxf(ma[0], mb[0]));
  Wts w;
  w.o = __builtin_amdgcn_exp2f(mo[0] - M); w.p = __builtin_amdgcn_exp2f(mp[0] - M);
  w.a = __builtin_amdgcn_exp2f(ma[0] - M); w.b = __builtin_amdgcn_exp2f(mb[0] - M);
  const float inv = 1.f / merge4(mo[1], w.o, mp[1], w.p, ma[1], w.a, mb[1], w.b);
  w.o *= inv; w.p *= inv; w.a *= inv; w.b *= inv;
  return w;
}
__device__ __forceinline__ u32x4 merge_piece(u32x4 xo, u32x4 xp, u32x4 xa, u32x4 xb, const Wts& w) {
  u32x4 r;
#pragma unroll
  for (int j = 0; j < 4; ++j)
    r[j] = pk_bf16(merge4(bf_lo(xo[j]), w.o, bf_lo(xp[j]), w.p, bf_lo(xa[j]), w.a, bf_lo(xb[j]), w.b),
                   merge4(bf_hi(xo[j]), w.o, bf_hi(xp[j]), w.p, bf_hi(xa[j]), w.a, bf_hi(xb[j]), w.b));
  return r;
}
__device__ __forceinline__ f32x2 ld_ml(rsrc_t rs, int lane, int rec) {
  return __builtin_bit_cast(f32x2, __builtin_amdgcn_raw_buffer_load_b64(rs, lane * 8 + PART_ML, rec, AUX_SC1));
}
__device__ __forceinline__ u32x4 ld_piece(rsrc_t rs, int lane, int e, int rec) {
  return __builtin_amdgcn_raw_buffer_load_b128(rs, lane * 16 + e * 1024, rec, AUX_SC1);
}

__global__ void __launch_bounds__(512, 2)
attn_kernel(const __hip_bfloat16* __restrict__ q, const int* __restrict__ qk, const int* __restrict__ qv,
            const __hip_bfloat16* __restrict__ ksc, const __hip_bfloat16* __restrict__ kzr,
            const __hip_bfloat16* __restrict__ vsc, const __hip_bfloat16* __restrict__ vzr,
            __hip_bfloat16* __restrict__ out, char* ws) {
  extern __shared__ __attribute__((aligned(16))) char lds[];
  const int tid = threadIdx.x, lane = tid & 63;
  const int wave = __builtin_amdgcn_readfirstlane(tid >> 6);
  const int bh = blockIdx.x & (NBH - 1), split = blockIdx.x >> 6;
  const int c = lane & 31, h = lane >> 5;
  const size_t kso = ((size_t)bh * NGK + (size_t)split * TPS) * DH * 2;
  const size_t vso = ((size_t)bh * SKV + (size_t)split * KPS) * 2;

  if (wave < 4) {
    __builtin_amdgcn_s_setprio(3);
    ProdCtx pc;
    const size_t base = ((size_t)bh * SKV + (size_t)split * KPS) * (DH * 4);
    pc.rk = mk_rsrc((const char*)qk + base, KPS * DH * 4);
    pc.rv = mk_rsrc((const char*)qv + base, KPS * DH * 4);
    pc.koff = wave * 8192 + lane * 16;
    pc.voff = wave * 8192 + h * 4096 + c * 16;
    pc.lds = lds;
    pc.kw = L_K + (16 * wave + h) * KROW + c * 8;
    pc.vw = L_V + wave * 4096 + lane * 16;
    pc.ksr = c * 8;
    pc.vsr = (16 * wave + 8 * h) * 2;
    Scales sc;
    sc.ks = ld_nt<u32x2>((const char*)ksc + kso + pc.ksr);
    sc.kz = ld_nt<u32x2>((const char*)kzr + kso + pc.ksr);
    sc.vs = ld_nt<u32x4>((const char*)vsc + vso + pc.vsr);
    sc.vz = ld_nt<u32x4>((const char*)vzr + vso + pc.vsr);
    SB();
    u32x4 rk[8];
#pragma unroll
    for (int i = 0; i < 8; ++i) rk[i] = ld_k(pc, 0, i);
    SB();
    unsigned tn = now_ns() + PACE0_NS;
    for (int k = 0; k < TPS; ++k) produce(pc, rk, sc, k, tn);
    asm volatile("s_waitcnt vmcnt(0)" ::: "memory");
  } else {
    const int n = wave - 4;
    const int ct = tid - 256;
    gu64* const ew = (gu64*)(ws + (bh * 4 + n) * GRP_BYTES);
    gu64* const cw = ew + 8;
    const u32x4 a0 = ld_nt<u32x4>((const char*)ksc + kso + ct * 16), b0 = ld_nt<u32x4>((const char*)kzr + kso + ct * 16);
    const u32x4 c0 = ld_nt<u32x4>((const char*)vsc + vso + ct * 16), d0 = ld_nt<u32x4>((const char*)vzr + vso + ct * 16);
    const u32x4 a1 = ld_nt<u32x4>((const char*)ksc + kso + 4096 + ct * 16), b1 = ld_nt<u32x4>((const char*)kzr + kso + 4096 + ct * 16);
    SB();
    u32x4 qraw[8];
    {
      const char* qp = (const char*)q + ((size_t)bh * 4 + n) * (SQ * DH * 2) + lane * 16;
#pragma unroll
      for (int i = 0; i < 8; ++i) qraw[i] = ld_nt<u32x4>(qp + i * 1024);
    }
    SB();
    u64 e0v = __hip_atomic_load(ew, RLX_AGENT);
    u64 c0v = __hip_atomic_load(cw, RLX_AGENT);
    SB();
    *(u32x4*)(lds + L_KS + ct * 16) = a0; *(u32x4*)(lds + L_KZ + ct * 16) = b0;
    *(u32x4*)(lds + L_VS + ct * 16) = c0; *(u32x4*)(lds + L_VZ + ct * 16) = d0;
    *(u32x4*)(lds + L_KS + 4096 + ct * 16) = a1; *(u32x4*)(lds + L_KZ + 4096 + ct * 16) = b1;
    bf16x8 qf[8];
    {
      char* qs = lds + L_Q + n * (SQ * KROW);
#pragma unroll
      for (int i = 0; i < 8; ++i) *(u32x4*)(qs + (4 * i + (lane >> 4)) * KROW + (lane & 15) * 16) = qraw[i];
      __builtin_amdgcn_fence(__ATOMIC_RELEASE, "wavefront");
      __builtin_amdgcn_wave_barrier();
      __builtin_amdgcn_fence(__ATOMIC_ACQUIRE, "wavefront");
#pragma unroll
      for (int s = 0; s < 8; ++s) qf[s] = *(const bf16x8*)(qs + c * KROW + h * 16 + s * 32);
    }
    const u64 epoch = uniform64(e0v);
    const u64 tag = ((epoch + 1) * 0x9E3779B97F4A7C15ull) & ~15ull;
    {
      u64 cur = uniform64(c0v);
      const bool mine = (cur & ~15ull) == tag && (cur & 15ull) != 15ull;
      if (!mine && lane == 0) __hip_atomic_compare_exchange_strong(cw, &cur, tag, __ATOMIC_RELAXED, RLX_AGENT);
    }

    const int pr = (c & 0x13) | ((c & 4) << 1) | ((c & 8) >> 1);
    const unsigned kr = L_K + pr * KROW + h * 16;
    const unsigned vr = L_V + lane * 16;
    ConsState st;
#pragma unroll
    for (int i = 0; i < 4; ++i)
#pragma unroll
      for (int g = 0; g < 16; ++g) st.o[i][g] = 0.f;
    st.m = -__builtin_inff(); st.l = 0.f;
    for (int k = 0; k < TPS; ++k) consume(st, lds, qf, kr, vr);

    const f32x2 ml = {st.m, xhalf_sum(st.l)};
    u32x4 own[8];
#pragma unroll
    for (int e = 0; e < 8; ++e)
#pragma unroll
      for (int b = 0; b < 2; ++b) {
        const int g = e * 2 + b;
        own[e][2 * b] = pk_bf16(st.o[0][g], st.o[1][g]);
        own[e][2 * b + 1] = pk_bf16(st.o[2][g], st.o[3][g]);
      }
    const rsrc_t rs = mk_rsrc(ws, WS_BYTES);
    const int cwo = (bh * 4 + n) * GRP_BYTES + 64;
    const bool owner = split == n;
    bool ready = false;
    if (owner) {
#pragma unroll
      for (int e = 0; e < 8; ++e) *(u32x4*)(lds + L_OWN + e * 1024 + lane * 16) = own[e];
      *(f32x2*)(lds + L_OWN + PART_ML + lane * 8) = ml;
      asm volatile("s_waitcnt vmcnt(0)" ::: "memory");
#pragma nounroll
      for (int spin = 0; spin < SPIN_CAP; ++spin) {
        asm volatile("" ::: "memory");
        if ((unsigned)__builtin_amdgcn_readfirstlane(__builtin_amdgcn_raw_buffer_load_b32(rs, cwo, 0, AUX_SC1)) == ((unsigned)tag | 3u)) { ready = true; break; }
        __builtin_amdgcn_s_sleep(1);
      }
      if (lane == 0) *(int*)(lds + L_OWN + PART_BYTES) = ready;
    }
    if (!ready) {
      const int rec0 = (bh * 4 + n) * GRP_BYTES + 128;
      const int rec = rec0 + split * PART_BYTES;
#pragma unroll
      for (int e = 0; e < 8; ++e) __builtin_amdgcn_raw_buffer_store_b128(own[e], rs, lane * 16 + e * 1024, rec, AUX_SC1);
      __builtin_amdgcn_raw_buffer_store_b64(__builtin_bit_cast(u32x2, ml), rs, lane * 8 + PART_ML, rec, AUX_SC1);
      asm volatile("s_waitcnt vmcnt(0)" ::: "memory");
      u64 old = 0;
      if (lane == 0) old = __hip_atomic_fetch_add(cw, owner ? 8ull : 1ull, RLX_AGENT);
      const unsigned o4 = (unsigned)__builtin_amdgcn_readfirstlane((unsigned)old) & 15u;
      if (__builtin_expect(o4 == (owner ? 3u : 10u), 0)) {
        __builtin_amdgcn_fence(__ATOMIC_ACQUIRE, "agent");
        const int rp = rec0 + (split ^ 1) * PART_BYTES, ra = rec0 + ((split & 2) ^ 2) * PART_BYTES, rb = ra + PART_BYTES;
        const Wts w = merge_weights(ml, ld_ml(rs, lane, rp), ld_ml(rs, lane, ra), ld_ml(rs, lane, rb));
        char* op = (char*)out + (size_t)((bh * 4 + n) * SQ + c) * (DH * 2) + h * 32;
#pragma unroll
        for (int e = 0; e < 8; ++e)
          *(u32x4*)(op + (e >> 1) * 64 + (e & 1) * 16) =
              merge_piece(own[e], ld_piece(rs, lane, e, rp), ld_piece(rs, lane, e, ra), ld_piece(rs, lane, e, rb), w);
        if (lane == 0) { __hip_atomic_store(cw, tag | 15ull, RLX_AGENT); __hip_atomic_store(ew, epoch + 1, RLX_AGENT); }
      }
    }
    __syncthreads();
    if (__builtin_amdgcn_readfirstlane(*(const int*)(lds + L_OWN + PART_BYTES))) {
      const int rec0 = (bh * 4 + split) * GRP_BYTES + 128;
      const int rp = rec0 + (split ^ 1) * PART_BYTES, ra = rec0 + ((split & 2) ^ 2) * PART_BYTES, rb = ra + PART_BYTES;
      const f32x2 mp = ld_ml(rs, lane, rp), ma = ld_ml(rs, lane, ra), mb = ld_ml(rs, lane, rb);
      const int e0 = 2 * n;
      const u32x4 xp0 = ld_piece(rs, lane, e0, rp), xa0 = ld_piece(rs, lane, e0, ra), xb0 = ld_piece(rs, lane, e0, rb);
      const u32x4 xp1 = ld_piece(rs, lane, e0 + 1, rp), xa1 = ld_piece(rs, lane, e0 + 1, ra), xb1 = ld_piece(rs, lane, e0 + 1, rb);
      const f32x2 mo = *(const f32x2*)(lds + L_OWN + PART_ML + lane * 8);
      const u32x4 xo0 = *(const u32x4*)(lds + L_OWN + e0 * 1024 + lane * 16);
      const u32x4 xo1 = *(const u32x4*)(lds + L_OWN + e0 * 1024 + 1024 + lane * 16);
      SB();
      const Wts w = merge_weights(mo, mp, ma, mb);
      char* op = (char*)out + (size_t)((bh * 4 + split) * SQ + c) * (DH * 2) + h * 32 + n * 64;
      *(u32x4*)op = merge_piece(xo0, xp0, xa0, xb0, w);
      *(u32x4*)(op + 16) = merge_piece(xo1, xp1, xa1, xb1, w);
      if (owner && lane == 0) { __hip_atomic_store(cw, tag | 15ull, RLX_AGENT); __hip_atomic_store(ew, epoch + 1, RLX_AGENT); }
    }
  }
}

}

extern "C" void kernel_launch(void* const* d_in, const int* in_sizes, int n_in, void* d_out, int out_size,
                              void* d_ws, size_t ws_size, hipStream_t stream) {
  using namespace kvq;
  (void)in_sizes; (void)n_in; (void)out_size; (void)ws_size;
  (void)hipFuncSetAttribute((const void*)attn_kernel, hipFuncAttributeMaxDynamicSharedMemorySize, LDS_BYTES);
  hipLaunchKernelGGL(attn_kernel, dim3(NBH * NSPLIT), dim3(512), LDS_BYTES, stream,
                     (const __hip_bfloat16*)d_in[0], (const int*)d_in[1], (const int*)d_in[2],
                     (const __hip_bfloat16*)d_in[3], (const __hip_bfloat16*)d_in[4],
                     (const __hip_bfloat16*)d_in[5], (const __hip_bfloat16*)d_in[6],
                     (__hip_bfloat16*)d_out, (char*)d_ws);
}
```
